```python
import torch

def _softmax(x, axis=-1):
    return torch.softmax(x, dim=axis)
def _gelu(x):
    return torch.nn.functional.gelu(x, approximate="tanh")
def _silu(x):
    return torch.nn.functional.silu(x)
def _sigmoid(x):
    return torch.sigmoid(x)
def _layernorm(x, axis=-1, eps=1e-5):
    m = x.mean(dim=axis, keepdim=True)
    v = x.var(dim=axis, keepdim=True, unbiased=False)
    return (x - m) / torch.sqrt(v + eps)
def _scatter_add(tgt, idx, src, axis=0):
    return tgt.clone().index_add_(axis, idx, src)
def _segment_sum(src, seg, n):
    out = torch.zeros((n,) + src.shape[1:], dtype=src.dtype)
    return out.index_add_(0, seg, src)
def _pad(x, pw):
    flat = tuple(v for pair in reversed(pw) for v in pair)
    return torch.nn.functional.pad(x, flat)

kernel_name = 'sequential_preLN__softmax_full_mha__mixer_token_mlp__prompt'

def setup_inputs():

    _w = (lambda a: (lambda t: t.to(torch.bfloat16) if t.is_floating_point() else t)(torch.as_tensor(a)))
    def _A_setup():
        B, S, D = 1, 2048, 576
        return {
            "x": _w((lambda *s: torch.randn(*s, dtype=torch.bfloat16) * 0.05)(B, S, D)),
            "wqkv": _w((lambda *s: torch.randn(*s, dtype=torch.bfloat16) * 0.05)(D, 3 * D)),
            "wo": _w((lambda *s: torch.randn(*s, dtype=torch.bfloat16) * 0.05)(D, D)),
        }
    _A_in = _A_setup()

    def _B_setup():
        B, S, D, F = 1, 2048, 576, 4608
        return {
            "x": _w((lambda *s: torch.randn(*s, dtype=torch.bfloat16) * 0.05)(B, S, D)),
            "w1": _w((lambda *s: torch.randn(*s, dtype=torch.bfloat16) * 0.8)(S, F) * (S ** -0.5)),
            "b1": _w((lambda *s: torch.randn(*s, dtype=torch.bfloat16) * 12.8)(F) * 0.02),
            "w2": _w((lambda *s: torch.randn(*s, dtype=torch.bfloat16) * 0.4)(F, S) * (F ** -0.5)),
            "b2": _w((lambda *s: torch.randn(*s, dtype=torch.bfloat16) * 6.4)(S) * 0.02),
        }
    _B_in = _B_setup()

    return {
        "A_x": _A_in["x"],
        "A_wqkv": _A_in["wqkv"],
        "A_wo": _A_in["wo"],
        "B_x": _B_in["x"],
        "B_w1": _B_in["w1"],
        "B_b1": _B_in["b1"],
        "B_w2": _B_in["w2"],
        "B_b2": _B_in["b2"],
    }

def reference(A_x, A_wqkv, A_wo, B_x, B_w1, B_b1, B_w2, B_b2):
    def _A_ref(x, wqkv, wo):
        B, S, D = x.shape
        H = 32; Dh = D // H
        qkv = (lambda x, *d: x.permute(*d))((x @ wqkv).reshape(B, S, 3, H, Dh), 2, 0, 3, 1, 4)
        q, k, v = qkv[0], qkv[1], qkv[2]
        scale = Dh ** -0.5
        scores = torch.einsum("bhqd,bhkd->bhqk", q, k) * scale
        p = _softmax(scores, axis=-1)
        o = torch.einsum("bhqk,bhkd->bhqd", p, v)
        return (lambda x, *d: x.permute(*d))(o, 0, 2, 1, 3).reshape(B, S, D) @ wo

    def _B_ref(x, w1, b1, w2, b2):
        B, S, D = x.shape
        h = _layernorm(x, axis=-1)
        u = torch.einsum("bsd,sf->bdf", h, w1) + b1
        u = _gelu(u)
        y = torch.einsum("bdf,fs->bds", u, w2) + b2
        return (lambda x, *d: x.permute(*d))(y, 0, 2, 1)

    x = A_x
    h = x + _A_ref(_layernorm(x, axis=-1), A_wqkv, A_wo)
    h2 = h + _B_ref(_layernorm(h, axis=-1), B_w1, B_b1, B_w2, B_b2)
    return h2
```

```cpp
#include <hip/hip_runtime.h>
#include <hip/hip_bf16.h>
#include <stdint.h>

typedef __attribute__((ext_vector_type(8))) short bf16x8;
typedef __attribute__((ext_vector_type(4))) short bf16x4;
typedef __attribute__((ext_vector_type(4))) float f32x4;
typedef __attribute__((ext_vector_type(2))) float f32x2;
typedef __attribute__((ext_vector_type(16))) float f32x16;
typedef __attribute__((ext_vector_type(4))) unsigned u32x4;
typedef __attribute__((ext_vector_type(2))) unsigned u32x2;
typedef __attribute__((ext_vector_type(2))) __bf16 bf2_t;
#define LDSP __attribute__((address_space(3)))
#define DI __device__ __forceinline__

constexpr int S_ = 2048, D_ = 576, H_ = 32, DH_ = 18, F_ = 4608;

constexpr size_t WS_QA = 0;
constexpr size_t WS_QB = WS_QA + 32u * 64 * 1024;
constexpr size_t KV_HEAD = 163840;
constexpr size_t WS_KV = WS_QB + 32u * 64 * 256;
constexpr size_t WS_KMAX = WS_KV + 32 * KV_HEAD;
constexpr size_t WS_O = WS_KMAX + 8192;
constexpr size_t WS_H = WS_O + (size_t)S_ * D_ * 2;
constexpr size_t WS_HNT = WS_H + (size_t)S_ * D_ * 2;
constexpr size_t WS_U = WS_HNT + (size_t)S_ * D_ * 2;
constexpr size_t WS_STAT = WS_U + (size_t)D_ * F_ * 2;
constexpr size_t WS_FLAG = WS_STAT + 131072;
constexpr size_t WS_P4 = WS_FLAG + 32768;
constexpr size_t WS_P5 = WS_P4 + 256u * 65536;
constexpr size_t WS_END = WS_P5 + 256u * 81920;

#define PROF_DECL
#define PROF(slot)
#define PROF_FLUSH(kid)
constexpr unsigned long long STAT_SENT = 0xFFFFFFFFFFFFFFFFull;

DI unsigned pack2(float lo, float hi) {
  f32x2 f = {lo, hi};
  bf2_t h = __builtin_convertvector(f, bf2_t);
  return __builtin_bit_cast(unsigned, h);
}
DI float bflo(unsigned u) { return __uint_as_float(u << 16); }
DI float bfhi(unsigned u) { return __uint_as_float(u & 0xffff0000u); }
DI bf16x4 trread(const char* lds_generic_unused, unsigned addr) {
  return __builtin_amdgcn_ds_read_tr16_b64_v4i16((LDSP bf16x4*)(uintptr_t)addr);
}
DI bf16x8 cat8(bf16x4 a, bf16x4 b) { return __builtin_shufflevector(a, b, 0, 1, 2, 3, 4, 5, 6, 7); }
DI unsigned lds_addr(const void* p) { return (unsigned)(uintptr_t)(LDSP const char*)p; }
#define MFMA16(a, b, c) __builtin_amdgcn_mfma_f32_16x16x32_bf16((a), (b), (c), 0, 0, 0)
#define MFMA32(a, b, c) __builtin_amdgcn_mfma_f32_32x32x16_bf16((a), (b), (c), 0, 0, 0)
#define MFMA32K8(a, b, c) __builtin_amdgcn_mfma_f32_32x32x8bf16_1k((a), (b), (c), 0, 0, 0)

template <int N> DI void wait_vm() { asm volatile("s_waitcnt vmcnt(%0)" ::"i"(N) : "memory"); }
DI void bar() { asm volatile("s_waitcnt lgkmcnt(0)" ::: "memory"); __builtin_amdgcn_s_barrier(); asm volatile("" ::: "memory"); }

DI void glds16(const void* gsrc, unsigned lds_dst) {
  unsigned keep;
  asm volatile("s_mov_b32 %0, m0\n\ts_mov_b32 m0, %2\n\ts_nop 0\n\tglobal_load_lds_dwordx4 %1, off\n\ts_mov_b32 m0, %0"
               : "=&s"(keep) : "v"(gsrc), "s"(__builtin_amdgcn_readfirstlane(lds_dst)) : "memory");
}
DI void gload16(u32x4& dst, const void* gsrc) {
  asm volatile("global_load_dwordx4 %0, %1, off" : "=v"(dst) : "v"(gsrc) : "memory");
}
DI void gload8(u32x2& dst, const void* gsrc) {
  asm volatile("global_load_dwordx2 %0, %1, off" : "=v"(dst) : "v"(gsrc) : "memory");
}
DI void gload4(unsigned& dst, const void* gsrc) {
  asm volatile("global_load_dword %0, %1, off" : "=v"(dst) : "v"(gsrc) : "memory");
}
DI int swap23(int r) { return (r & ~12) | ((r & 4) << 1) | ((r & 8) >> 1); }

constexpr int K1_RS = 224;
constexpr int K1_T2RS = 264;
__global__ __launch_bounds__(512) void k1_qkv(const unsigned short* __restrict__ x, const unsigned short* __restrict__ wqkv,
                                              char* __restrict__ ws) {
  __shared__ __attribute__((aligned(16))) char smem[576 * K1_RS + 512];
  const int b = blockIdx.x, xg = b & 7, idx = b >> 3;
  const int rb = (xg >> 1) * 4 + (idx >> 3);
  const int ct = (xg & 1) * 8 + (idx & 7);
  const int tid = threadIdx.x, lane = tid & 63;
  const int w = __builtin_amdgcn_readfirstlane(tid >> 6);
  const int r = lane & 15, g = lane >> 4;
  const int win0 = ct * 108 - (ct & 1) * 4;
  const int coff = (ct & 1) * 4;
  const unsigned sbase = lds_addr(smem);
  PROF_DECL PROF(0);

  {
    int gt = b * 512 + tid;
    if (gt < 16384) ((unsigned long long*)(ws + WS_STAT))[gt] = STAT_SENT;
    else if (gt < 16384 + 8192) ((unsigned*)(ws + WS_FLAG))[gt - 16384] = 0u;
  }

  u32x4 a[18];
  const char* arow = (const char*)(x + (size_t)(rb * 128 + w * 16 + r) * D_ + g * 8);
  const char* wb = (const char*)(wqkv + win0);
  auto issueB = [&](int i) {
    int p = i * 8 + w; p = p > 125 ? 125 : p;
    int c = p * 64 + lane;
    int rho = c / 14, cc = c - rho * 14;
    int k = swap23(rho);
    glds16(wb + (size_t)k * (3 * D_ * 2) + cc * 16, sbase + p * 1024);
  };
  gload16(a[0], arow); gload16(a[1], arow + 64); gload16(a[2], arow + 128);
  issueB(0); issueB(1); issueB(2);
  gload16(a[3], arow + 192); gload16(a[4], arow + 256); gload16(a[5], arow + 320);
  issueB(3); issueB(4); issueB(5);
  gload16(a[6], arow + 384); gload16(a[7], arow + 448); gload16(a[8], arow + 512);
  issueB(6); issueB(7);
  gload16(a[9], arow + 576); gload16(a[10], arow + 640); gload16(a[11], arow + 704);
  issueB(8); issueB(9); issueB(10);
  gload16(a[12], arow + 768); gload16(a[13], arow + 832); gload16(a[14], arow + 896);
  issueB(11); issueB(12); issueB(13);
  gload16(a[15], arow + 960); gload16(a[16], arow + 1024); gload16(a[17], arow + 1088);
  issueB(14); issueB(15);

  f32x4 acc[7], accs = {0, 0, 0, 0}, accq = {0, 0, 0, 0}, accc = {0, 0, 0, 0};
#pragma unroll
  for (int j = 0; j < 7; ++j) acc[j] = f32x4{0, 0, 0, 0};
  const bf16x8 ones = {0x3f80, 0x3f80, 0x3f80, 0x3f80, 0x3f80, 0x3f80, 0x3f80, 0x3f80};
  const int q4 = (lane & 15) >> 2, p4 = lane & 3;
  const unsigned bbase = sbase + (16 * (g >> 1) + 4 * (g & 1) + q4) * K1_RS + p4 * 8;
  const int cw = w < 7 ? w : 6;

#define K1_STEP(j)                                                                                     \
  {                                                                                                    \
    bf16x8 af = __builtin_bit_cast(bf16x8, a[j]);                                                      \
    _Pragma("unroll") for (int c = 0; c < 7; ++c) {                                                    \
      bf16x8 bf = cat8(trread(smem, bbase + (j) * 32 * K1_RS + c * 32), trread(smem, bbase + (j) * 32 * K1_RS + 8 * K1_RS + c * 32)); \
      acc[c] = MFMA16(af, bf, acc[c]);                                                                 \
    }                                                                                                  \
    {                                                                                                  \
      bf16x8 bf = cat8(trread(smem, bbase + (j) * 32 * K1_RS + cw * 32), trread(smem, bbase + (j) * 32 * K1_RS + 8 * K1_RS + cw * 32)); \
      accc = MFMA16(ones, bf, accc);                                                                   \
    }                                                                                                  \
    accs = MFMA16(af, ones, accs);                                                                     \
    accq = MFMA16(af, af, accq);                                                                       \
  }
#define K1_PHASE(ph, NW)                                                                               \
  asm volatile("s_waitcnt vmcnt(%3)" : "+v"(a[3 * ph]), "+v"(a[3 * ph + 1]), "+v"(a[3 * ph + 2]) : "i"(NW) : "memory"); \
  __builtin_amdgcn_s_barrier();                                                                        \
  asm volatile("" ::: "memory");                                                                       \
  K1_STEP(3 * ph) K1_STEP(3 * ph + 1) K1_STEP(3 * ph + 2)
  PROF(1);
  K1_PHASE(0, 28)
  PROF(2);
  K1_PHASE(1, 22)
  K1_PHASE(2, 17)
  K1_PHASE(3, 11)
  K1_PHASE(4, 5)
  PROF(3);
  K1_PHASE(5, 0)
  PROF(4);
#undef K1_STEP
#undef K1_PHASE

  bar();
  float* csum = (float*)(smem + 576 * K1_RS);
  if (w < 7 && g == 0) csum[w * 16 + r] = accc[0];
  float mean[4], rstd[4];
#pragma unroll
  for (int i = 0; i < 4; ++i) {
    float sq = __shfl(accq[i], g * 16 + 4 * g + i);
    float m = accs[i] * (1.0f / D_);
    float var = sq * (1.0f / D_) - m * m;
    var = fmaxf(var, 0.f);
    mean[i] = m; rstd[i] = rsqrtf(var + 1e-5f);
  }
  bar();
#pragma unroll
  for (int c = 0; c < 7; ++c) {
    int col = c * 16 + r;
    float cs = csum[col];
    int gcol = win0 + col;
    float sc = gcol < D_ ? 0.34004502f   : 1.0f;
    float v0 = (acc[c][0] - mean[0] * cs) * rstd[0] * sc, v1 = (acc[c][1] - mean[1] * cs) * rstd[1] * sc;
    float v2 = (acc[c][2] - mean[2] * cs) * rstd[2] * sc, v3 = (acc[c][3] - mean[3] * cs) * rstd[3] * sc;
    u32x2 pk = {pack2(v0, v1), pack2(v2, v3)};
    *(u32x2*)(smem + col * K1_T2RS + (w * 16 + 4 * g) * 2) = pk;
  }
  bar();
  if (w < 6) {
    const int slice = ct * 6 + w;
    const int type = slice >> 5, head = slice & 31;
    const int c0 = coff + 18 * w;
    if (type < 2) {
      const int hh = g >> 1;
      char* dstA = type == 0 ? ws + WS_QA + ((size_t)head * 64 + rb * 4) * 1024 : ws + WS_KV + head * KV_HEAD + rb * 9984;
      char* dstB = type == 0 ? ws + WS_QB + ((size_t)head * 64 + rb * 4) * 256 : ws + WS_KV + head * KV_HEAD + rb * 9984 + 4096;
      float nmax = 0.f;
#pragma unroll
      for (int t = 0; t < 4; ++t) {
        unsigned ad = sbase + (c0 + 8 * hh + q4) * K1_T2RS + (t * 32 + (g & 1) * 16 + 4 * p4) * 2;
        bf16x8 v = cat8(trread(smem, ad), trread(smem, ad + 4 * K1_T2RS));
        u32x4 u = __builtin_bit_cast(u32x4, v);
        *(u32x4*)(dstA + t * 1024 + lane * 16) = u;
        float n = 0.f;
#pragma unroll
        for (int e = 0; e < 4; ++e) { float lo = bflo(u[e]), hi = bfhi(u[e]); n += lo * lo + hi * hi; }
        nmax = fmaxf(nmax, n);
      }
      float nmb = 0.f;
#pragma unroll
      for (int t = 0; t < 2; ++t) {
        unsigned ad = sbase + (c0 + 16 + q4) * K1_T2RS + (t * 64 + g * 16 + 4 * p4) * 2;
        bf16x4 v = trread(smem, ad);
        u32x2 u = __builtin_bit_cast(u32x2, v);
        u[1] = type == 1 ? 0x00003f80u : 0u;
        *(u32x2*)(dstB + t * 512 + lane * 8) = u;
        float lo = bflo(u[0]), hi = bfhi(u[0]);
        nmb = fmaxf(nmb, lo * lo + hi * hi);
      }
      if (type == 1) {
#pragma unroll
        for (int o = 1; o < 32; o <<= 1) nmax = fmaxf(nmax, __shfl_xor(nmax, o));
#pragma unroll
        for (int o = 1; o < 64; o <<= 1) nmb = fmaxf(nmb, __shfl_xor(nmb, o));
        float* km = (float*)(ws + WS_KMAX) + head * 48;
        if (lane == 0) { km[rb] = nmax; km[32 + rb] = nmb; }
        if (lane == 32) km[16 + rb] = nmax;
      }
    } else {
      char* dst = ws + WS_KV + head * KV_HEAD + rb * 9984 + 5120;
#pragma unroll
      for (int it = 0; it < 5; ++it) {
        int pi = it * 64 + lane;
        if (pi < 304) {
          int s = pi / 38, rem = pi - s * 38, hh = rem >= 19 ? 1 : 0, d = rem - hh * 19;
          u32x4 u;
          if (d == 18) { u = u32x4{0x3f803f80u, 0x3f803f80u, 0x3f803f80u, 0x3f803f80u}; }
          else {
            const char* src = smem + (c0 + d) * K1_T2RS + (s * 16 + 4 * hh) * 2;
            u32x2 lo = *(const u32x2*)src, hi = *(const u32x2*)(src + 16);
            u = u32x4{lo[0], lo[1], hi[0], hi[1]};
          }
          *(u32x4*)(dst + pi * 16) = u;
        }
      }
    }
  }
  PROF(5); PROF_FLUSH(1);
}

__global__ __launch_bounds__(512) void k2_attn(char* __restrict__ ws) {
  __shared__ __attribute__((aligned(16))) char smem[163840];
  const int b = blockIdx.x, xg = b & 7, idx = b >> 3;
  const int head = xg * 4 + (idx >> 3), qblk = idx & 7;
  const int tid = threadIdx.x, lane = tid & 63;
  const int w = __builtin_amdgcn_readfirstlane(tid >> 6);
  const int r = lane & 31, hh = lane >> 5;
  const int qt = qblk * 8 + w;
  const unsigned sbase = lds_addr(smem);
  PROF_DECL PROF(0);

  u32x4 qa_u; u32x2 qb_u; unsigned km_u;
  gload16(qa_u, ws + WS_QA + (((size_t)head * 64 + qt) * 64 + lane) * 16);
  gload8(qb_u, ws + WS_QB + (((size_t)head * 64 + qt) * 32 + r) * 8);
  gload4(km_u, ws + WS_KMAX + (head * 48 + (lane < 48 ? lane : 0)) * 4);
  const char* kv = ws + WS_KV + head * KV_HEAD;
#pragma unroll
  for (int i = 0; i < 20; ++i) {
    int p = i * 8 + w; p = p > 155 ? 155 : p;
    glds16(kv + p * 1024 + lane * 16, sbase + p * 1024);
  }
  asm volatile("s_waitcnt vmcnt(20)" : "+v"(qa_u), "+v"(qb_u), "+v"(km_u)::"memory");
  float km = __uint_as_float(km_u);
#pragma unroll
  for (int o = 1; o < 16; o <<= 1) km = fmaxf(km, __shfl_xor(km, o));
  float kn2 = __shfl(km, 0) + __shfl(km, 16) + __shfl(km, 32);
  float qn2 = 0.f;
#pragma unroll
  for (int e = 0; e < 4; ++e) { float lo = bflo(qa_u[e]), hi = bfhi(qa_u[e]); qn2 += lo * lo + hi * hi; }
  if (hh == 0) { float lo = bflo(qb_u[0]), hi = bfhi(qb_u[0]); qn2 += lo * lo + hi * hi; }
  qn2 += __shfl_xor(qn2, 32);
  const float mb = sqrtf(qn2 * kn2);
  u32x2 qbm;
  qbm[0] = hh == 0 ? qb_u[0] : 0u;
  qbm[1] = hh == 0 ? pack2(-mb, 0.f) : 0u;
  const bf16x8 qa = __builtin_bit_cast(bf16x8, qa_u);
  const bf16x4 qb = __builtin_bit_cast(bf16x4, qbm);

  const int dsel = r < 19 ? r : ((r >= 20 && r < 28) ? 0 : 4);
  const char* ka_p = smem + lane * 16;
  const char* kb_p = smem + 4096 + r * 8;
  const char* v_p = smem + 5120 + (hh * 19 + dsel) * 16;
  f32x16 o;
#pragma unroll
  for (int i = 0; i < 16; ++i) o[i] = 0.f;
  const f32x16 zero16 = o;

  PROF(1);
  for (int gq = 0; gq < 16; ++gq) {
    if (gq == 0) { wait_vm<18>(); bar(); PROF(2); }
    else if (gq == 1) { wait_vm<17>(); bar(); }
    else if (gq == 2) { wait_vm<15>(); bar(); }
    else if (gq == 4) { wait_vm<10>(); bar(); PROF(3); }
    else if (gq == 8) { wait_vm<0>(); bar(); PROF(4); }
    const int go = gq * 9984;
#pragma unroll
    for (int t = 0; t < 4; ++t) {
      bf16x8 ka = *(const bf16x8*)(ka_p + go + t * 1024);
      bf16x4 kb = *(const bf16x4*)(kb_p + go + t * 256);
      bf16x8 v0 = *(const bf16x8*)(v_p + go + (2 * t) * 608);
      bf16x8 v1 = *(const bf16x8*)(v_p + go + (2 * t + 1) * 608);
      f32x16 s = MFMA32(ka, qa, zero16);
      s = MFMA32K8(kb, qb, s);
      u32x4 p0, p1;
#pragma unroll
      for (int e = 0; e < 4; ++e) {
        p0[e] = pack2(__builtin_amdgcn_exp2f(s[2 * e]), __builtin_amdgcn_exp2f(s[2 * e + 1]));
        p1[e] = pack2(__builtin_amdgcn_exp2f(s[8 + 2 * e]), __builtin_amdgcn_exp2f(s[8 + 2 * e + 1]));
      }
      o = MFMA32(v0, __builtin_bit_cast(bf16x8, p0), o);
      o = MFMA32(v1, __builtin_bit_cast(bf16x8, p1), o);
    }
  }
  PROF(5);
  float l = __shfl(o[10], r);
  if (__builtin_expect(__any(!(l >= 5.421e-20f)), 0)) {
    u32x2 qz; qz[0] = qbm[0]; qz[1] = 0u;
    const bf16x4 qb0 = __builtin_bit_cast(bf16x4, qz);
    float mx = -3.0e38f;
    for (int t = 0; t < 64; ++t) {
      const int go = (t >> 2) * 9984;
      bf16x8 ka = *(const bf16x8*)(ka_p + go + (t & 3) * 1024);
      bf16x4 kb = *(const bf16x4*)(kb_p + go + (t & 3) * 256);
      f32x16 s = MFMA32(ka, qa, zero16);
      s = MFMA32K8(kb, qb0, s);
#pragma unroll
      for (int e = 0; e < 16; ++e) mx = fmaxf(mx, s[e]);
    }
    mx = fmaxf(mx, __shfl_xor(mx, 32));
    o = zero16;
    for (int t = 0; t < 64; ++t) {
      const int go = (t >> 2) * 9984;
      bf16x8 ka = *(const bf16x8*)(ka_p + go + (t & 3) * 1024);
      bf16x4 kb = *(const bf16x4*)(kb_p + go + (t & 3) * 256);
      bf16x8 v0 = *(const bf16x8*)(v_p + go + (2 * (t & 3)) * 608);
      bf16x8 v1 = *(const bf16x8*)(v_p + go + (2 * (t & 3) + 1) * 608);
      f32x16 s = MFMA32(ka, qa, zero16);
      s = MFMA32K8(kb, qb0, s);
      u32x4 p0, p1;
#pragma unroll
      for (int e = 0; e < 4; ++e) {
        p0[e] = pack2(__builtin_amdgcn_exp2f(s[2 * e] - mx), __builtin_amdgcn_exp2f(s[2 * e + 1] - mx));
        p1[e] = pack2(__builtin_amdgcn_exp2f(s[8 + 2 * e] - mx), __builtin_amdgcn_exp2f(s[8 + 2 * e + 1] - mx));
      }
      o = MFMA32(v0, __builtin_bit_cast(bf16x8, p0), o);
      o = MFMA32(v1, __builtin_bit_cast(bf16x8, p1), o);
    }
    l = __shfl(o[10], r);
  }
  const float rl = 1.0f / l;
  unsigned* orow = (unsigned*)(ws + WS_O + ((size_t)(qt * 32 + r) * D_ + head * DH_) * 2);
  orow[2 * hh] = pack2(o[0] * rl, o[1] * rl);
  orow[2 * hh + 1] = pack2(o[2] * rl, o[3] * rl);
  orow[4 + 2 * hh] = pack2(o[4] * rl, o[5] * rl);
  orow[4 + 2 * hh + 1] = pack2(o[6] * rl, o[7] * rl);
  if (hh == 0) orow[8] = pack2(o[8] * rl, o[9] * rl);
  PROF(6); PROF_FLUSH(2);
}

constexpr int K3_RS = 160;
__global__ __launch_bounds__(256) void k3_oproj(const unsigned short* __restrict__ x, const unsigned short* __restrict__ wo,
                                                char* __restrict__ ws) {
  __shared__ __attribute__((aligned(16))) char smem[576 * K3_RS + 2048];
  const int b = blockIdx.x, xg = b & 7, idx = b >> 3;
  const int rb = xg * 4 + (idx >> 3), ct = idx & 7;
  const int tid = threadIdx.x, lane = tid & 63;
  const int w = __builtin_amdgcn_readfirstlane(tid >> 6);
  const int r = lane & 15, g = lane >> 4;
  const int win0 = ct == 7 ? 496 : ct * 72;
  const int coff = ct == 7 ? 8 : 0;
  const unsigned sbase = lds_addr(smem);
  const int token = rb * 64 + w * 16 + r;
  PROF_DECL PROF(0);

  u32x4 a[18]; u32x2 xr[5];
  const char* arow = ws + WS_O + ((size_t)token * D_ + g * 8) * 2;
  const char* wb = (const char*)(wo + win0);
  auto issueB = [&](int i) {
    int p = i * 4 + w; p = p > 89 ? 89 : p;
    int c = p * 64 + lane;
    int rho = c / 10, cc = c - rho * 10;
    int k = swap23(rho);
    glds16(wb + (size_t)k * (D_ * 2) + cc * 16, sbase + p * 1024);
  };
  const char* xrow = (const char*)(x + (size_t)token * D_ + win0 + 4 * g);
#pragma unroll
  for (int c = 0; c < 5; ++c) gload8(xr[c], xrow + c * 32);
  gload16(a[0], arow); gload16(a[1], arow + 64); gload16(a[2], arow + 128);
  issueB(0); issueB(1); issueB(2); issueB(3);
  gload16(a[3], arow + 192); gload16(a[4], arow + 256); gload16(a[5], arow + 320);
  issueB(4); issueB(5); issueB(6); issueB(7);
  gload16(a[6], arow + 384); gload16(a[7], arow + 448); gload16(a[8], arow + 512);
  issueB(8); issueB(9); issueB(10); issueB(11);
  gload16(a[9], arow + 576); gload16(a[10], arow + 640); gload16(a[11], arow + 704);
  issueB(12); issueB(13); issueB(14);
  gload16(a[12], arow + 768); gload16(a[13], arow + 832); gload16(a[14], arow + 896);
  issueB(15); issueB(16); issueB(17); issueB(18);
  gload16(a[15], arow + 960); gload16(a[16], arow + 1024); gload16(a[17], arow + 1088);
  issueB(19); issueB(20); issueB(21); issueB(22);

  f32x4 acc[5];
#pragma unroll
  for (int j = 0; j < 5; ++j) acc[j] = f32x4{0, 0, 0, 0};
  const int q4 = (lane & 15) >> 2, p4 = lane & 3;
  const unsigned bbase = sbase + (16 * (g >> 1) + 4 * (g & 1) + q4) * K3_RS + p4 * 8;
#define K3_STEP(j)                                                                                     \
  {                                                                                                    \
    bf16x8 af = __builtin_bit_cast(bf16x8, a[j]);                                                      \
    _Pragma("unroll") for (int c = 0; c < 5; ++c) {                                                    \
      bf16x8 bf = cat8(trread(smem, bbase + (j) * 32 * K3_RS + c * 32), trread(smem, bbase + (j) * 32 * K3_RS + 8 * K3_RS + c * 32)); \
      acc[c] = MFMA16(bf, af, acc[c]);                        \
    }                                                                                                  \
  }
#define K3_PHASE(ph, NW)                                                                               \
  asm volatile("s_waitcnt vmcnt(%3)" : "+v"(a[3 * ph]), "+v"(a[3 * ph + 1]), "+v"(a[3 * ph + 2]) : "i"(NW) : "memory"); \
  __builtin_amdgcn_s_barrier();                                                                        \
  asm volatile("" ::: "memory");                                                                       \
  K3_STEP(3 * ph) K3_STEP(3 * ph + 1) K3_STEP(3 * ph + 2)
  PROF(1);
  K3_PHASE(0, 34)
  PROF(2);
  K3_PHASE(1, 27)
  K3_PHASE(2, 20)
  K3_PHASE(3, 14)
  K3_PHASE(4, 7)
  K3_PHASE(5, 0)
  PROF(3);
#undef K3_STEP
#undef K3_PHASE
  asm volatile("" : "+v"(xr[0]), "+v"(xr[1]), "+v"(xr[2]), "+v"(xr[3]), "+v"(xr[4]));
  float hv[5][4];
  float sum = 0.f;
  unsigned short* hrow = (unsigned short*)(ws + WS_H) + (size_t)token * D_ + win0 + 4 * g;
#pragma unroll
  for (int c = 0; c < 5; ++c) {
    int col = c * 16 + 4 * g;
    bool valid = col >= coff && col < coff + 72;
    unsigned p0 = pack2(bflo(xr[c][0]) + acc[c][0], bfhi(xr[c][0]) + acc[c][1]);
    unsigned p1 = pack2(bflo(xr[c][1]) + acc[c][2], bfhi(xr[c][1]) + acc[c][3]);
    hv[c][0] = bflo(p0); hv[c][1] = bfhi(p0); hv[c][2] = bflo(p1); hv[c][3] = bfhi(p1);
    if (valid) {
      *(u32x2*)(hrow + c * 16) = u32x2{p0, p1};
      sum += (hv[c][0] + hv[c][1]) + (hv[c][2] + hv[c][3]);
    }
  }
  sum += __shfl_xor(sum, 16); sum += __shfl_xor(sum, 32);
  const float lm = sum * (1.0f / 72);
  float m2 = 0.f;
#pragma unroll
  for (int c = 0; c < 5; ++c) {
    int col = c * 16 + 4 * g;
    bool valid = col >= coff && col < coff + 72;
    if (valid) {
#pragma unroll
      for (int i = 0; i < 4; ++i) { float d = hv[c][i] - lm; m2 += d * d; }
    }
  }
  m2 += __shfl_xor(m2, 16); m2 += __shfl_xor(m2, 32);
  unsigned long long* st = (unsigned long long*)(ws + WS_STAT) + (size_t)rb * 512;
  {
    unsigned long long v = ((unsigned long long)__float_as_uint(m2) << 32) | __float_as_uint(lm);
    if (v == STAT_SENT) v = 0x7fc000007fc00000ull;
    if (g == 0) __hip_atomic_store(st + ct * 64 + w * 16 + r, v, __ATOMIC_RELAXED, __HIP_MEMORY_SCOPE_AGENT);
  }
  unsigned long long s0, s1;
  {
    const unsigned long long* p0 = st + (2 * g) * 64 + w * 16 + r;
    const unsigned long long* p1 = p0 + 64;
    for (unsigned spin = 0; spin < (1u << 24); ++spin) {
      s0 = __hip_atomic_load(p0, __ATOMIC_RELAXED, __HIP_MEMORY_SCOPE_AGENT);
      s1 = __hip_atomic_load(p1, __ATOMIC_RELAXED, __HIP_MEMORY_SCOPE_AGENT);
      if (__all(s0 != STAT_SENT && s1 != STAT_SENT)) break;
      __builtin_amdgcn_s_sleep(1);
    }
  }
  PROF(4);
  float ma = __uint_as_float((unsigned)s0), mb = __uint_as_float((unsigned)s1);
  float qa = __uint_as_float((unsigned)(s0 >> 32)), qb = __uint_as_float((unsigned)(s1 >> 32));
  float msum = ma + mb;
  msum += __shfl_xor(msum, 16); msum += __shfl_xor(msum, 32);
  const float mean = msum * 0.125f;
  float dv = (qa + qb) + 72.f * ((ma - mean) * (ma - mean) + (mb - mean) * (mb - mean));
  dv += __shfl_xor(dv, 16); dv += __shfl_xor(dv, 32);
  const float var = dv * (1.0f / D_);
  const float r1 = rsqrtf(var + 1e-5f);
  const float r2 = rsqrtf(var * r1 * r1 + 1e-5f);
  const float rr = r1 * r2;
  bar();
  constexpr int TRS = 168;
#pragma unroll
  for (int c = 0; c < 5; ++c) {
    u32x2 pk = {pack2((hv[c][0] - mean) * rr, (hv[c][1] - mean) * rr), pack2((hv[c][2] - mean) * rr, (hv[c][3] - mean) * rr)};
    *(u32x2*)(smem + (w * 16 + r) * TRS + (c * 16 + 4 * g) * 2) = pk;
  }
  bar();
  for (int it = w; it < 10; it += 4) {
    const int kk = it / 5, c = it - kk * 5;
    unsigned ad = sbase + (kk * 32 + 8 * g + q4) * TRS + (c * 16 + 4 * p4) * 2;
    bf16x8 v = cat8(trread(smem, ad), trread(smem, ad + 4 * TRS));
    int col = c * 16 + r;
    if (col >= coff && col < coff + 72) {
      int d = win0 + col;
      int mblk = d / 144, dd = d - mblk * 144;
      int k32 = rb * 2 + kk;
      char* dst = ws + WS_HNT + ((((size_t)mblk * 64 + k32) * 9 + (dd >> 4)) * 64 + g * 16 + (dd & 15)) * 16;
      *(u32x4*)dst = __builtin_bit_cast(u32x4, v);
    }
  }
  PROF(5); PROF_FLUSH(3);
}

template <int MODE> struct GC;
template <> struct GC<4> { static constexpr int NT = 9, BRS = 288, NST = 16, KSTEPS_TOT = 64, BPITCH = F_ * 2; };
template <> struct GC<5> { static constexpr int NT = 8, BRS = 256, NST = 18, KSTEPS_TOT = 144, BPITCH = S_ * 2; };

template <int MODE, int NR, int NC>
DI void gemm_wave(const char* smem, unsigned sbase, int w, int lane, int rt0, int ct0, const char* (&src)[5], const unsigned (&dsto)[5],
                  const int (&stride)[5], int nload, f32x4 (&acc)[5][3]) {
  using C = GC<MODE>;
  constexpr int SLOT = 18432 + 64 * C::BRS;
  const int g = lane >> 4, q4 = (lane & 15) >> 2, p4 = lane & 3;
  const unsigned abase = sbase + rt0 * 1024 + lane * 16;
  unsigned bb[NC];
  {
    const int rho = 16 * (g >> 1) + 4 * (g & 1) + q4;
#pragma unroll
    for (int j = 0; j < NC; ++j) {
      if (MODE == 4) bb[j] = sbase + 18432 + rho * C::BRS + (ct0 + j) * 32 + p4 * 8;
      else bb[j] = sbase + 18432 + rho * C::BRS + (((ct0 + j) ^ (rho & 7)) * 32) + p4 * 8;
    }
  }
  auto issue = [&](int s) {
    const unsigned so = sbase + (s & 3) * SLOT;
#pragma unroll
    for (int i = 0; i < 4; ++i) { glds16(src[i], so + dsto[i]); src[i] += stride[i]; }
    if (nload == 5) { glds16(src[4], so + dsto[4]); src[4] += stride[4]; }
  };
  issue(0); issue(1); issue(2);
#pragma unroll 1
  for (int s = 0; s < C::NST; ++s) {
    if (s < C::NST - 2) { if (nload == 5) wait_vm<10>(); else wait_vm<8>(); }
    else if (s == C::NST - 2) { if (nload == 5) wait_vm<5>(); else wait_vm<4>(); }
    else wait_vm<0>();
    bar();
    if (s + 3 < C::NST) issue(s + 3);
    const unsigned so = (s & 3) * SLOT;
#pragma unroll
    for (int kk = 0; kk < 2; ++kk) {
      bf16x8 af[NR], bfr[NC];
#pragma unroll
      for (int i = 0; i < NR; ++i) af[i] = *(const LDSP bf16x8*)(uintptr_t)(abase + so + kk * 9216 + i * 1024);
#pragma unroll
      for (int j = 0; j < NC; ++j)
        bfr[j] = cat8(trread(smem, bb[j] + so + kk * 32 * C::BRS), trread(smem, bb[j] + so + kk * 32 * C::BRS + 8 * C::BRS));
#pragma unroll
      for (int i = 0; i < NR; ++i)
#pragma unroll
        for (int j = 0; j < NC; ++j) {
          if (MODE == 4) acc[i][j] = MFMA16(bfr[j], af[i], acc[i][j]);
          else acc[i][j] = MFMA16(af[i], bfr[j], acc[i][j]);
        }
    }
  }
}

DI float gelu_tanh(float x) {
  float u = x * (0.7978845608028654f + 0.0356774081363001f * x * x);
  float e = __builtin_amdgcn_exp2f(-2.8853900817779268f * u);
  return x * __builtin_amdgcn_rcpf(1.0f + e);
}

DI void flag_wait(const unsigned* f) {
  for (unsigned spin = 0; spin < (1u << 24); ++spin) {
    unsigned v = __hip_atomic_load(f, __ATOMIC_RELAXED, __HIP_MEMORY_SCOPE_AGENT);
    if (v != 0u) break;
    __builtin_amdgcn_s_sleep(1);
  }
}

template <int MODE>
DI void gemm_setup(int w, int lane, const char* Aimg, const char* Bmat, const char* (&src)[5], unsigned (&dsto)[5], int (&stride)[5], int& nload) {
  using C = GC<MODE>;
  constexpr int NBP = 64 * C::BRS / 1024;
  constexpr int CPR = C::BRS / 16;
  if (MODE == 4) nload = w < 4 ? 4 : 5; else nload = (w == 4 || w == 5) ? 5 : 4;
#pragma unroll
  for (int i = 0; i < 5; ++i) {
    int q;
    bool isA;
    int piece;
    if (w < 4) { isA = true; piece = 4 * w + (i < 4 ? i : 3); }
    else {
      if (MODE == 4) q = (w - 4) * 5 + i;
      else q = w < 6 ? (w - 4) * 5 + i : 10 + (w - 6) * 4 + (i < 4 ? i : 3);
      isA = q < 2; piece = isA ? 16 + q : q - 2;
    }
    if (isA) { src[i] = Aimg + piece * 1024 + lane * 16; dsto[i] = piece * 1024; stride[i] = 18432; }
    else {
      int c = piece * 64 + lane;
      int rho = c / CPR, cc = c - rho * CPR;
      if (MODE == 5) cc ^= (rho & 7) << 1;
      int k = swap23(rho);
      src[i] = Bmat + (size_t)k * C::BPITCH + cc * 16; dsto[i] = 18432 + piece * 1024; stride[i] = 64 * C::BPITCH;
    }
  }
  (void)NBP;
}

template <int NR, int NC>
DI void k4_epilogue(int b, int kh, int w, int lane, int mblk, int ntile, int rt0, int ct0, f32x4 (&acc)[5][3], const unsigned short* b1,
                    char* ws) {
  const int r = lane & 15, g = lane >> 4;
  auto rs = __builtin_amdgcn_make_buffer_rsrc(ws + WS_P4, 0, 256 * 65536, 0x00020000);
#pragma unroll
  for (int i = 0; i < NR; ++i)
#pragma unroll
    for (int j = 0; j < NC; ++j) {
      const int t = i * NC + j;
      if ((t & 1) != kh)
        __builtin_amdgcn_raw_buffer_store_b128(__builtin_bit_cast(u32x4, acc[i][j]), rs, b * 65536 + w * 8192 + (t >> 1) * 1024 + lane * 16, 0, 16);
    }
  wait_vm<0>();
  bar();
  unsigned* flags = (unsigned*)(ws + WS_FLAG);
  if (w == 0) {
    if (lane == 0) __hip_atomic_store(flags + b * 16, 1u, __ATOMIC_RELAXED, __HIP_MEMORY_SCOPE_AGENT);
    flag_wait(flags + (b ^ 128) * 16);
  }
  bar();
  const int pb = b ^ 128;
#pragma unroll
  for (int i = 0; i < NR; ++i)
#pragma unroll
    for (int j = 0; j < NC; ++j) {
      const int t = i * NC + j;
      if ((t & 1) == kh) {
        u32x4 pv = __builtin_amdgcn_raw_buffer_load_b128(rs, pb * 65536 + w * 8192 + (t >> 1) * 1024 + lane * 16, 0, 16);
        f32x4 v = acc[i][j] + __builtin_bit_cast(f32x4, pv);
        const int f = ntile * 144 + (ct0 + j) * 16 + 4 * g;
        u32x2 bb = *(const u32x2*)(b1 + f);
        float y0 = gelu_tanh(v[0] + bflo(bb[0])), y1 = gelu_tanh(v[1] + bfhi(bb[0]));
        float y2 = gelu_tanh(v[2] + bflo(bb[1])), y3 = gelu_tanh(v[3] + bfhi(bb[1]));
        const int k32 = f >> 5, gp = (f & 31) >> 3, half = (f >> 2) & 1;
        char* dst = ws + WS_U + ((((size_t)mblk * 144 + k32) * 9 + rt0 + i) * 64 + gp * 16 + r) * 16 + half * 8;
        *(u32x2*)dst = u32x2{pack2(y0, y1), pack2(y2, y3)};
      }
    }
}

__global__ __launch_bounds__(512) void k4_gemm1(const unsigned short* __restrict__ w1, const unsigned short* __restrict__ b1,
                                                char* __restrict__ ws) {
  __shared__ __attribute__((aligned(16))) char smem[4 * (18432 + 64 * 288)];
  const int b = blockIdx.x, xg = b & 7, idx = b >> 3;
  const int ntile = xg * 4 + (idx & 3), mblk = (idx >> 2) & 3, kh = idx >> 4;
  const int tid = threadIdx.x, lane = tid & 63;
  const int w = __builtin_amdgcn_readfirstlane(tid >> 6);
  const unsigned sbase = lds_addr(smem);
  const char* src[5]; unsigned dsto[5]; int stride[5]; int nload;
  PROF_DECL PROF(0);
  const char* Aimg = ws + WS_HNT + ((size_t)mblk * 64 + kh * 32) * 9216;
  const char* Bmat = (const char*)w1 + (size_t)(kh * 1024) * (F_ * 2) + ntile * 288;
  gemm_setup<4>(w, lane, Aimg, Bmat, src, dsto, stride, nload);
  f32x4 acc[5][3];
#pragma unroll
  for (int i = 0; i < 5; ++i)
#pragma unroll
    for (int j = 0; j < 3; ++j) acc[i][j] = f32x4{0, 0, 0, 0};
  const int wr = w >> 2, wc = w & 3;
  if (wr == 0) {
    if (wc == 0) { PROF(1); gemm_wave<4, 5, 3>(smem, sbase, w, lane, 0, 0, src, dsto, stride, nload, acc); PROF(2); k4_epilogue<5, 3>(b, kh, w, lane, mblk, ntile, 0, 0, acc, b1, ws); PROF(3); PROF_FLUSH(4); }
    else { const int c0 = 1 + 2 * wc; gemm_wave<4, 5, 2>(smem, sbase, w, lane, 0, c0, src, dsto, stride, nload, acc); k4_epilogue<5, 2>(b, kh, w, lane, mblk, ntile, 0, c0, acc, b1, ws); }
  } else {
    const int c0 = 2 * wc;
    if (wc == 3) { gemm_wave<4, 4, 3>(smem, sbase, w, lane, 5, c0, src, dsto, stride, nload, acc); k4_epilogue<4, 3>(b, kh, w, lane, mblk, ntile, 5, c0, acc, b1, ws); }
    else { gemm_wave<4, 4, 2>(smem, sbase, w, lane, 5, c0, src, dsto, stride, nload, acc); k4_epilogue<4, 2>(b, kh, w, lane, mblk, ntile, 5, c0, acc, b1, ws); }
  }
}

template <int NR>
DI void k5_epilogue(int b, int kq, int w, int lane, int mblk, int ntile, int rt0, int ct0, f32x4 (&acc)[5][3], const unsigned short* b2,
                    char* ws, unsigned short* out) {
  const int r = lane & 15, g = lane >> 4;
  auto rs = __builtin_amdgcn_make_buffer_rsrc(ws + WS_P5, 0, 256 * 81920, 0x00020000);
  constexpr int HALF = (NR + 1) / 2;
#pragma unroll
  for (int i = 0; i < NR; ++i)
#pragma unroll
    for (int j = 0; j < 2; ++j) {
      const int owner = j * 2 + (i < HALF ? 0 : 1);
      if (owner != kq)
        __builtin_amdgcn_raw_buffer_store_b128(__builtin_bit_cast(u32x4, acc[i][j]), rs, b * 81920 + w * 10240 + (i * 2 + j) * 1024 + lane * 16, 0, 16);
    }
  wait_vm<0>();
  bar();
  unsigned* flags = (unsigned*)(ws + WS_FLAG) + 256 * 16;
  const int b0 = b & 63;
  if (w == 0) {
    if (lane == 0) __hip_atomic_store(flags + b * 16, 1u, __ATOMIC_RELAXED, __HIP_MEMORY_SCOPE_AGENT);
    for (unsigned spin = 0; spin < (1u << 24); ++spin) {
      unsigned v = __hip_atomic_load(flags + (b0 + 64 * (lane & 3)) * 16, __ATOMIC_RELAXED, __HIP_MEMORY_SCOPE_AGENT);
      if (__all(v != 0u)) break;
      __builtin_amdgcn_s_sleep(1);
    }
  }
  bar();
  const unsigned short* h = (const unsigned short*)(ws + WS_H);
#pragma unroll
  for (int i = 0; i < NR; ++i)
#pragma unroll
    for (int j = 0; j < 2; ++j) {
      const int owner = j * 2 + (i < HALF ? 0 : 1);
      if (owner == kq) {
        f32x4 v = acc[i][j];
        u32x4 pv[3];
#pragma unroll
        for (int k = 0; k < 3; ++k) {
          const int pb = b0 + 64 * ((kq + 1 + k) & 3);
          pv[k] = __builtin_amdgcn_raw_buffer_load_b128(rs, pb * 81920 + w * 10240 + (i * 2 + j) * 1024 + lane * 16, 0, 16);
        }
        const int s = ntile * 128 + (ct0 + j) * 16 + r;
        const int d = mblk * 144 + (rt0 + i) * 16 + 4 * g;
        u32x2 hh = *(const u32x2*)(h + (size_t)s * D_ + d);
        float bias = __uint_as_float(((unsigned)b2[s]) << 16);
#pragma unroll
        for (int k = 0; k < 3; ++k) v += __builtin_bit_cast(f32x4, pv[k]);
        u32x2 o = {pack2(bflo(hh[0]) + (v[0] + bias), bfhi(hh[0]) + (v[1] + bias)), pack2(bflo(hh[1]) + (v[2] + bias), bfhi(hh[1]) + (v[3] + bias))};
        *(u32x2*)(out + (size_t)s * D_ + d) = o;
      }
    }
}

__global__ __launch_bounds__(512) void k5_gemm2(const unsigned short* __restrict__ w2, const unsigned short* __restrict__ b2,
                                                char* __restrict__ ws, unsigned short* __restrict__ out) {
  __shared__ __attribute__((aligned(16))) char smem[4 * (18432 + 64 * 256)];
  const int b = blockIdx.x, xg = b & 7, idx = b >> 3;
  const int ntile = xg * 2 + (idx & 1), mblk = (idx >> 1) & 3, kq = idx >> 3;
  const int tid = threadIdx.x, lane = tid & 63;
  const int w = __builtin_amdgcn_readfirstlane(tid >> 6);
  const unsigned sbase = lds_addr(smem);
  const char* src[5]; unsigned dsto[5]; int stride[5]; int nload;
  PROF_DECL PROF(0);
  const char* Aimg = ws + WS_U + ((size_t)mblk * 144 + kq * 36) * 9216;
  const char* Bmat = (const char*)w2 + (size_t)(kq * 1152) * (S_ * 2) + ntile * 256;
  gemm_setup<5>(w, lane, Aimg, Bmat, src, dsto, stride, nload);
  f32x4 acc[5][3];
#pragma unroll
  for (int i = 0; i < 5; ++i)
#pragma unroll
    for (int j = 0; j < 3; ++j) acc[i][j] = f32x4{0, 0, 0, 0};
  const int wr = w >> 2, wc = w & 3;
  if (wr == 0) { PROF(1); gemm_wave<5, 5, 2>(smem, sbase, w, lane, 0, 2 * wc, src, dsto, stride, nload, acc); PROF(2); k5_epilogue<5>(b, kq, w, lane, mblk, ntile, 0, 2 * wc, acc, b2, ws, out); PROF(3); PROF_FLUSH(5); }
  else { gemm_wave<5, 4, 2>(smem, sbase, w, lane, 5, 2 * wc, src, dsto, stride, nload, acc); k5_epilogue<4>(b, kq, w, lane, mblk, ntile, 5, 2 * wc, acc, b2, ws, out); }
}

extern "C" void kernel_launch(void* const* d_in, const int* in_sizes, int n_in, void* d_out, int out_size, void* d_ws, size_t ws_size,
                              hipStream_t stream) {
  const unsigned short* x = (const unsigned short*)d_in[0];
  const unsigned short* wqkv = (const unsigned short*)d_in[1];
  const unsigned short* wo = (const unsigned short*)d_in[2];
  const unsigned short* w1 = (const unsigned short*)d_in[4];
  const unsigned short* b1 = (const unsigned short*)d_in[5];
  const unsigned short* w2 = (const unsigned short*)d_in[6];
  const unsigned short* b2 = (const unsigned short*)d_in[7];
  char* ws = (char*)d_ws;
  k1_qkv<<<256, 512, 0, stream>>>(x, wqkv, ws);
  k2_attn<<<256, 512, 0, stream>>>(ws);
  k3_oproj<<<256, 256, 0, stream>>>(x, wo, ws);
  k4_gemm1<<<256, 512, 0, stream>>>(w1, b1, ws);
  k5_gemm2<<<256, 512, 0, stream>>>(w2, b2, ws, (unsigned short*)d_out);
}
```

```cpp
#include <hip/hip_runtime.h>
#include <hip/hip_bf16.h>
#include <stdint.h>

typedef __attribute__((ext_vector_type(8))) short bf16x8;
typedef __attribute__((ext_vector_type(4))) short bf16x4;
typedef __attribute__((ext_vector_type(4))) float f32x4;
typedef __attribute__((ext_vector_type(2))) float f32x2;
typedef __attribute__((ext_vector_type(16))) float f32x16;
typedef __attribute__((ext_vector_type(4))) unsigned u32x4;
typedef __attribute__((ext_vector_type(2))) unsigned u32x2;
typedef __attribute__((ext_vector_type(2))) __bf16 bf2_t;
#define LDSP __attribute__((address_space(3)))
#define DI __device__ __forceinline__

constexpr int S_ = 2048, D_ = 576, H_ = 32, DH_ = 18, F_ = 4608;

constexpr size_t WS_QA = 0;
constexpr size_t WS_QB = WS_QA + 32u * 64 * 1024;
constexpr size_t KV_HEAD = 163840;
constexpr size_t WS_KV = WS_QB + 32u * 64 * 256;
constexpr size_t WS_KMAX = WS_KV + 32 * KV_HEAD;
constexpr size_t WS_O = WS_KMAX + 8192;
constexpr size_t WS_H = WS_O + (size_t)S_ * D_ * 2;
constexpr size_t WS_HNT = WS_H + (size_t)S_ * D_ * 2;
constexpr size_t WS_U = WS_HNT + (size_t)S_ * D_ * 2;
constexpr size_t WS_STAT = WS_U + (size_t)D_ * F_ * 2;
constexpr size_t WS_P5 = WS_STAT + 262144;
constexpr int P5_SLOT = 8 * 3 * 1536;
constexpr size_t WS_END = WS_P5 + 256u * P5_SLOT;

#define PROF_DECL
#define PROF(slot)
#define PROF_FLUSH(kid)
#define PROF_ARGS
#define PROF_PASS
constexpr unsigned long long STAT_SENT = 0xFFFFFFFFFFFFFFFFull;

DI unsigned pack2(float lo, float hi) {
  f32x2 f = {lo, hi};
  bf2_t h = __builtin_convertvector(f, bf2_t);
  return __builtin_bit_cast(unsigned, h);
}
DI float bflo(unsigned u) { return __uint_as_float(u << 16); }
DI float bfhi(unsigned u) { return __uint_as_float(u & 0xffff0000u); }
DI float dot2(unsigned a, unsigned b, float c) {
  return __builtin_amdgcn_fdot2_f32_bf16(__builtin_bit_cast(bf2_t, a), __builtin_bit_cast(bf2_t, b), c, false);
}
DI bf16x4 trread(unsigned addr) { return __builtin_amdgcn_ds_read_tr16_b64_v4i16((LDSP bf16x4*)(uintptr_t)addr); }
DI bf16x8 ldsread16(unsigned addr) { return *(const LDSP bf16x8*)(uintptr_t)addr; }
DI bf16x4 ldsread8(unsigned addr) { return *(const LDSP bf16x4*)(uintptr_t)addr; }
DI bf16x8 cat8(bf16x4 a, bf16x4 b) { return __builtin_shufflevector(a, b, 0, 1, 2, 3, 4, 5, 6, 7); }
DI unsigned lds_addr(const void* p) { return (unsigned)(uintptr_t)(LDSP const char*)p; }
#define MFMA16(a, b, c) __builtin_amdgcn_mfma_f32_16x16x32_bf16((a), (b), (c), 0, 0, 0)
#define MFMA32(a, b, c) __builtin_amdgcn_mfma_f32_32x32x16_bf16((a), (b), (c), 0, 0, 0)
#define MFMA32K8(a, b, c) __builtin_amdgcn_mfma_f32_32x32x8bf16_1k((a), (b), (c), 0, 0, 0)
#define SBAR() __builtin_amdgcn_sched_barrier(0)

template <int N> DI void wait_vm() { asm volatile("s_waitcnt vmcnt(%0)" ::"i"(N) : "memory"); }
DI void bar() { asm volatile("" ::: "memory"); __builtin_amdgcn_s_waitcnt(0xC07F); __builtin_amdgcn_s_barrier(); asm volatile("" ::: "memory"); }
DI void rawbar() { asm volatile("" ::: "memory"); __builtin_amdgcn_s_barrier(); asm volatile("" ::: "memory"); }

DI void glds16(const char* ubase, unsigned voff, unsigned lds_dst) {
  asm volatile("s_mov_b32 m0, %2\n\ts_nop 0\n\tglobal_load_lds_dwordx4 %0, %1" ::"v"(voff), "s"(ubase), "s"(lds_dst) : "memory");
}
DI void touch(const char* ubase, unsigned voff, unsigned lds_dst) {
  asm volatile("s_mov_b32 m0, %2\n\ts_nop 0\n\tglobal_load_lds_dword %0, %1" ::"v"(voff), "s"(ubase), "s"(lds_dst) : "memory");
}
DI void gload16(u32x4& dst, const void* gsrc) { asm volatile("global_load_dwordx4 %0, %1, off" : "=v"(dst) : "v"(gsrc) : "memory"); }
DI void gload8(u32x2& dst, const void* gsrc) { asm volatile("global_load_dwordx2 %0, %1, off" : "=v"(dst) : "v"(gsrc) : "memory"); }
DI void gload4(unsigned& dst, const void* gsrc) { asm volatile("global_load_dword %0, %1, off" : "=v"(dst) : "v"(gsrc) : "memory"); }
DI int swap23(int r) { return (r & ~12) | ((r & 4) << 1) | ((r & 8) >> 1); }

template <class C>
DI void gemm_loader(unsigned sbase, int j, int lane, const char* abase, const char* bbase, const unsigned (&oa)[C::LA], const unsigned (&ob)[C::LB]) {
  constexpr int L = C::LA + C::LB;
  auto issue = [&](unsigned so) {
#pragma unroll
    for (int i = 0; i < C::LA; ++i) {
      if (i == C::LA - 1 && C::LASTA < 64) { if (lane < C::LASTA) glds16(abase, oa[i], sbase + so + i * 4096 + j * (C::LASTA * 16)); }
      else glds16(abase, oa[i], sbase + so + i * 4096 + j * 1024);
    }
#pragma unroll
    for (int i = 0; i < C::LB; ++i) {
      if (i == C::LB - 1 && C::LASTB < 64) { if (lane < C::LASTB) glds16(bbase, ob[i], sbase + so + C::ABYTES + i * 4096 + j * (C::LASTB * 16)); }
      else glds16(bbase, ob[i], sbase + so + C::ABYTES + i * 4096 + j * 1024);
    }
    abase += C::SA; bbase += C::SB;
  };
  constexpr int D = C::DEPTH;
  static_assert(D <= C::NSLOT - 2 && D >= 1 && D * L <= 63, "");
  unsigned so = 0;
#pragma unroll 1
  for (int s = 0; s < D; ++s) { issue(so); so += C::SLOT; }
#pragma unroll 1
  for (int k = 0; k < C::NST - D; ++k) {
    wait_vm<(D - 1) * L>();
    __builtin_amdgcn_s_barrier();
    issue(so);
    so = so + C::SLOT == C::NSLOT * C::SLOT ? 0u : so + C::SLOT;
  }
#pragma unroll
  for (int t = 0; t < D; ++t) {
    if (t == 0) wait_vm<(D - 1) * L>();
    if (t == 1) wait_vm<(D > 2 ? D - 2 : 0) * L>();
    if (t == 2) wait_vm<(D > 3 ? D - 3 : 0) * L>();
    if (t == 3) wait_vm<(D > 4 ? D - 4 : 0) * L>();
    if (t >= 4) wait_vm<0>();
    __builtin_amdgcn_s_barrier();
  }
}

template <class C, int NR, int NC, int STATS, int AR, int AC>
DI void gemm_core(unsigned sbase, int lane, int rt0, const unsigned (&bb)[NC], f32x4 (&acc)[AR][AC], f32x4 (&st)[5]) {
  const int r = lane & 15, g = lane >> 4;
  unsigned a0, a1;
  if (C::AMODE == 0) { a0 = sbase + rt0 * 1024 + lane * 16; a1 = a0 + C::NRT * 1024; }
  else { a0 = sbase + (rt0 * 16 + r) * 128 + ((g ^ (r & 7)) * 16); a1 = sbase + (rt0 * 16 + r) * 128 + (((4 + g) ^ (r & 7)) * 16); }
  constexpr int ASTEP = C::AMODE == 0 ? 1024 : 2048;
  const bf16x8 ones = {0x3f80, 0x3f80, 0x3f80, 0x3f80, 0x3f80, 0x3f80, 0x3f80, 0x3f80};
  bf16x8 fa0[NR], fb0[NC], fa1[NR], fb1[NC];
  auto readf = [&](bf16x8 (&fa)[NR], bf16x8 (&fb)[NC], unsigned so, int kk) {
    fa[0] = ldsread16((kk ? a1 : a0) + so);
#pragma unroll
    for (int j = 0; j < NC; ++j) fb[j] = cat8(trread(bb[j] + so + kk * 32 * C::BRS), trread(bb[j] + so + kk * 32 * C::BRS + 8 * C::BRS));
#pragma unroll
    for (int i = 1; i < NR; ++i) fa[i] = ldsread16((kk ? a1 : a0) + so + i * ASTEP);
  };
  auto mma = [&](bf16x8 (&fa)[NR], bf16x8 (&fb)[NC]) {
#pragma unroll
    for (int i = 0; i < NR; ++i)
#pragma unroll
      for (int j = 0; j < NC; ++j) {
        if (C::SWAP) acc[i][j] = MFMA16(fb[j], fa[i], acc[i][j]);
        else acc[i][j] = MFMA16(fa[i], fb[j], acc[i][j]);
      }
    if (STATS >= 1) st[4] = MFMA16(ones, fb[0], st[4]);
    if (STATS >= 2) {
#pragma unroll
      for (int i = 0; i < 2; ++i) { st[i] = MFMA16(fa[i], ones, st[i]); st[2 + i] = MFMA16(fa[i], fa[i], st[2 + i]); }
    }
  };
  constexpr int NRD = NR + 2 * NC, NMM = NR * NC + (STATS >= 1 ? 1 : 0) + (STATS >= 2 ? 4 : 0);
  constexpr int RPM = (NRD + NMM - 1) / NMM;
  auto ilv = [&]() {
#pragma unroll
    for (int k = 0; k < (NRD + RPM - 1) / RPM; ++k) {
      __builtin_amdgcn_sched_group_barrier(0x008, 1, 0);
      __builtin_amdgcn_sched_group_barrier(0x100, RPM, 0);
    }
    __builtin_amdgcn_sched_group_barrier(0x008, 32, 0);
  };
  rawbar();
  readf(fa0, fb0, 0, 0);
  SBAR();
  unsigned so = 0;
#pragma unroll 1
  for (int s = 0; s < C::NST - 1; ++s) {
    readf(fa1, fb1, so, 1);
    mma(fa0, fb0);
    ilv();
    SBAR();
    so = so + C::SLOT == C::NSLOT * C::SLOT ? 0u : so + C::SLOT;
    rawbar();
    SBAR();
    readf(fa0, fb0, so, 0);
    mma(fa1, fb1);
    ilv();
    SBAR();
  }
  readf(fa1, fb1, so, 1);
  mma(fa0, fb0);
  ilv();
  SBAR();
  mma(fa1, fb1);
}

#define K1_DEPTH 3
struct C1 { static constexpr int NST = 9, NSLOT = 5, DEPTH = K1_DEPTH, ABYTES = 16384, BRS = 224, SLOT = ABYTES + 64 * BRS, AMODE = 1, NRT = 8;
            static constexpr int LA = 4, LASTA = 64, LB = 4, LASTB = 32, SA = 128, SB = 64 * 3 * D_ * 2;
            static constexpr bool SWAP = false; };
constexpr int K1_T2RS = 264;
__global__ __launch_bounds__(768) void k1_qkv(const unsigned short* __restrict__ x, const unsigned short* __restrict__ wqkv,
                                              char* __restrict__ ws) {
  __shared__ __attribute__((aligned(16))) char smem[C1::NSLOT * C1::SLOT + 1536];
  const int b = blockIdx.x, xg = b & 7, idx = b >> 3;
  const int rb = (xg >> 1) * 4 + (idx >> 3);
  const int ct = (xg & 1) * 8 + (idx & 7);
  const int tid = threadIdx.x, lane = tid & 63;
  const int w = __builtin_amdgcn_readfirstlane(tid >> 6);
  const int r = lane & 15, g = lane >> 4;
  const int win0 = ct * 108 - (ct & 1) * 4;
  const int coff = (ct & 1) * 4;
  const unsigned sbase = lds_addr(smem);
  PROF_DECL PROF(0);

  if (w >= 8) {
    const int j = w - 8;
    unsigned oa[4], ob[4];
#pragma unroll
    for (int i = 0; i < 4; ++i) {
      const int row = (i * 4 + j) * 8 + (lane >> 3);
      oa[i] = row * (D_ * 2) + (((lane & 7) ^ (row & 7)) * 16);
    }
#pragma unroll
    for (int i = 0; i < 4; ++i) {
      const int c = i < 3 ? (i * 4 + j) * 64 + lane : 768 + j * 32 + (lane & 31);
      const int rho = c / 14, cc = c - rho * 14;
      ob[i] = swap23(rho) * (3 * D_ * 2) + cc * 16;
    }
    PROF(1);
    gemm_loader<C1>(sbase, j, lane, (const char*)(x + (size_t)rb * 128 * D_), (const char*)(wqkv + win0), oa, ob);
    PROF(2); PROF_FLUSH(1);
    return;
  }
  {
    const int gt = b * 512 + tid;
    if (gt < 32768) ((unsigned long long*)(ws + WS_STAT))[gt] = STAT_SENT;
  }
  float* csum = (float*)(smem + C1::NSLOT * C1::SLOT);
  float* rstat = (float*)(smem + C1::NSLOT * C1::SLOT + 512);
  const int wm = w & 3, wn = w >> 2;
  const int q4 = (lane & 15) >> 2, p4 = lane & 3;
  const unsigned bbase = sbase + C1::ABYTES + (16 * (g >> 1) + 4 * (g & 1) + q4) * C1::BRS + p4 * 8;
  f32x4 acc[2][4], st[5];
#pragma unroll
  for (int i = 0; i < 2; ++i)
#pragma unroll
    for (int j = 0; j < 4; ++j) acc[i][j] = f32x4{0, 0, 0, 0};
#pragma unroll
  for (int i = 0; i < 5; ++i) st[i] = f32x4{0, 0, 0, 0};
  int ctile[4];
  if (wn == 0) {
    unsigned bb[4];
#pragma unroll
    for (int j = 0; j < 4; ++j) { ctile[j] = (wm + j) & 3; bb[j] = bbase + ctile[j] * 32; }
    gemm_core<C1, 2, 4, 1>(sbase, lane, wm * 2, bb, acc, st);
  } else {
    unsigned bb[3];
#pragma unroll
    for (int j = 0; j < 3; ++j) { int c = (wm == 3 ? 0 : wm) + j; c = c >= 3 ? c - 3 : c; ctile[j] = 4 + c; bb[j] = bbase + ctile[j] * 32; }
    ctile[3] = 7;
    gemm_core<C1, 2, 3, 2>(sbase, lane, wm * 2, bb, acc, st);
    const int e = r - 4 * g;
#pragma unroll
    for (int i = 0; i < 2; ++i) {
      float q0 = st[2 + i][0], q1 = st[2 + i][1], q2 = st[2 + i][2], q3 = st[2 + i][3];
      float s0 = st[i][0], s1 = st[i][1], s2 = st[i][2], s3 = st[i][3];
      asm volatile("" : "+v"(q0), "+v"(q1), "+v"(q2), "+v"(q3), "+v"(s0), "+v"(s1), "+v"(s2), "+v"(s3));
      const float sq = (e & 2) ? ((e & 1) ? q3 : q2) : ((e & 1) ? q1 : q0);
      const float sm = (e & 2) ? ((e & 1) ? s3 : s2) : ((e & 1) ? s1 : s0);
      const float m = sm * (1.0f / D_);
      const float var = fmaxf(sq * (1.0f / D_) - m * m, 0.f);
      if (e >= 0 && e < 4) *(f32x2*)(rstat + ((wm * 2 + i) * 16 + r) * 2) = f32x2{m, __builtin_amdgcn_rsqf(var + 1e-5f)};
    }
  }
  PROF(1);
  if (g == 0) csum[ctile[0] * 16 + r] = st[4][0];
  bar();
#pragma unroll
  for (int i = 0; i < 2; ++i) {
    const f32x4 s01 = *(const f32x4*)(rstat + ((wm * 2 + i) * 16 + 4 * g) * 2), s23 = *(const f32x4*)(rstat + ((wm * 2 + i) * 16 + 4 * g + 2) * 2);
#pragma unroll
    for (int j = 0; j < 4; ++j) {
      if (j < 3 || wn == 0) {
        const int col = ctile[j] * 16 + r;
        const float c_ = csum[col];
        const float sc = win0 + col < D_ ? 0.34004502f   : 1.0f;
        float v0 = (acc[i][j][0] - s01[0] * c_) * (s01[1] * sc), v1 = (acc[i][j][1] - s01[2] * c_) * (s01[3] * sc);
        float v2 = (acc[i][j][2] - s23[0] * c_) * (s23[1] * sc), v3 = (acc[i][j][3] - s23[2] * c_) * (s23[3] * sc);
        *(u32x2*)(smem + col * K1_T2RS + ((wm * 2 + i) * 16 + 4 * g) * 2) = u32x2{pack2(v0, v1), pack2(v2, v3)};
      }
    }
  }
  bar();
  PROF(2);
  if (w < 6) {
    const int slice = ct * 6 + w;
    const int type = slice >> 5, head = slice & 31;
    const int c0 = coff + 18 * w;
    if (type < 2) {
      const int hh = g >> 1;
      char* dstA = type == 0 ? ws + WS_QA + ((size_t)head * 64 + rb * 4) * 1024 : ws + WS_KV + head * KV_HEAD + rb * 9984;
      char* dstB = type == 0 ? ws + WS_QB + ((size_t)head * 64 + rb * 4) * 256 : ws + WS_KV + head * KV_HEAD + rb * 9984 + 4096;
      u32x4 u[4]; u32x2 ub[2];
      const unsigned ad = sbase + (c0 + 8 * hh + q4) * K1_T2RS + ((g & 1) * 16 + 4 * p4) * 2;
      const unsigned adb = sbase + (c0 + 16 + q4) * K1_T2RS + (g * 16 + 4 * p4) * 2;
#pragma unroll
      for (int t = 0; t < 4; ++t) u[t] = __builtin_bit_cast(u32x4, cat8(trread(ad + t * 64), trread(ad + t * 64 + 4 * K1_T2RS)));
#pragma unroll
      for (int t = 0; t < 2; ++t) { ub[t] = __builtin_bit_cast(u32x2, trread(adb + t * 128)); ub[t][1] = type == 1 ? 0x00003f80u : 0u; }
#pragma unroll
      for (int t = 0; t < 4; ++t) *(u32x4*)(dstA + t * 1024 + lane * 16) = u[t];
#pragma unroll
      for (int t = 0; t < 2; ++t) *(u32x2*)(dstB + t * 512 + lane * 8) = ub[t];
      if (type == 1) {
        float nmax = 0.f;
#pragma unroll
        for (int t = 0; t < 4; ++t) nmax = fmaxf(nmax, dot2(u[t][0], u[t][0], dot2(u[t][1], u[t][1], dot2(u[t][2], u[t][2], dot2(u[t][3], u[t][3], 0.f)))));
        float nmb = fmaxf(dot2(ub[0][0], ub[0][0], 0.f), dot2(ub[1][0], ub[1][0], 0.f));
#pragma unroll
        for (int o = 1; o < 32; o <<= 1) { nmax = fmaxf(nmax, __shfl_xor(nmax, o)); nmb = fmaxf(nmb, __shfl_xor(nmb, o)); }
        nmb = fmaxf(nmb, __shfl_xor(nmb, 32));
        float* km = (float*)(ws + WS_KMAX) + head * 48;
        if (lane == 0) { km[rb] = nmax; km[32 + rb] = nmb; }
        if (lane == 32) km[16 + rb] = nmax;
      }
    } else {
      char* dst = ws + WS_KV + head * KV_HEAD + rb * 9984 + 5120;
      u32x4 u[5];
#pragma unroll
      for (int it = 0; it < 5; ++it) {
        int pi = it * 64 + lane; pi = pi < 304 ? pi : 0;
        const int s = pi / 38, rem = pi - s * 38, hh = rem >= 19 ? 1 : 0, d = rem - hh * 19;
        const char* src = smem + (c0 + (d == 18 ? 0 : d)) * K1_T2RS + (s * 16 + 4 * hh) * 2;
        const u32x2 lo = *(const u32x2*)src, hi = *(const u32x2*)(src + 16);
        u[it] = d == 18 ? u32x4{0x3f803f80u, 0x3f803f80u, 0x3f803f80u, 0x3f803f80u} : u32x4{lo[0], lo[1], hi[0], hi[1]};
      }
#pragma unroll
      for (int it = 0; it < 5; ++it)
        if (it < 4 || lane < 48) *(u32x4*)(dst + (it * 64 + lane) * 16) = u[it];
    }
  }
  PROF(3); PROF_FLUSH(1);
}

#define W2_K2 5
DI unsigned exp2pk(float a, float b) { return pack2(__builtin_amdgcn_exp2f(a), __builtin_amdgcn_exp2f(b)); }

DI void k2_exact(const char* kvg, int lane, bf16x8 qa, bf16x4 qb0, f32x16& o) {
  const int r = lane & 31, hh = lane >> 5;
  const char* ka_p = kvg + lane * 16;
  const char* kb_p = kvg + 4096 + r * 8;
  const char* v_p = kvg + 5120 + (hh * 19 + (r < 19 ? r : 0)) * 16;
  f32x16 zero16;
#pragma unroll
  for (int i = 0; i < 16; ++i) zero16[i] = 0.f;
  float mx = -3.0e38f;
#pragma unroll 1
  for (int t = 0; t < 64; ++t) {
    const int go = (t >> 2) * 9984;
    f32x16 s = MFMA32(*(const bf16x8*)(ka_p + go + (t & 3) * 1024), qa, zero16);
    s = MFMA32K8(*(const bf16x4*)(kb_p + go + (t & 3) * 256), qb0, s);
#pragma unroll
    for (int e = 0; e < 16; ++e) mx = fmaxf(mx, s[e]);
  }
  mx = fmaxf(mx, __shfl_xor(mx, 32));
  o = zero16;
#pragma unroll 1
  for (int t = 0; t < 64; ++t) {
    const int go = (t >> 2) * 9984;
    bf16x8 v0 = *(const bf16x8*)(v_p + go + (2 * (t & 3)) * 608);
    bf16x8 v1 = *(const bf16x8*)(v_p + go + (2 * (t & 3) + 1) * 608);
    f32x16 s = MFMA32(*(const bf16x8*)(ka_p + go + (t & 3) * 1024), qa, zero16);
    s = MFMA32K8(*(const bf16x4*)(kb_p + go + (t & 3) * 256), qb0, s);
    u32x4 p0, p1;
#pragma unroll
    for (int e = 0; e < 4; ++e) {
      p0[e] = exp2pk(s[2 * e] - mx, s[2 * e + 1] - mx);
      p1[e] = exp2pk(s[8 + 2 * e] - mx, s[8 + 2 * e + 1] - mx);
    }
    o = MFMA32(v0, __builtin_bit_cast(bf16x8, p0), o);
    o = MFMA32(v1, __builtin_bit_cast(bf16x8, p1), o);
  }
}

__global__ __launch_bounds__(768) void k2_attn(char* __restrict__ ws, const unsigned short* __restrict__ wo, const unsigned short* __restrict__ w1,
                                               const unsigned short* __restrict__ w2) {
  __shared__ __attribute__((aligned(16))) char smem[163840];
  const int b = blockIdx.x, xg = b & 7, idx = b >> 3;
  const int head = xg * 4 + (idx >> 3), qblk = idx & 7;
  const int tid = threadIdx.x, lane = tid & 63;
  const int w = __builtin_amdgcn_readfirstlane(tid >> 6);
  const int r = lane & 31, hh = lane >> 5;
  const unsigned sbase = lds_addr(smem);
  const char* kvg = ws + WS_KV + head * KV_HEAD;
  volatile LDSP unsigned* xflag = (volatile LDSP unsigned*)(uintptr_t)(sbase + 163328);
  PROF_DECL PROF(0);

  if (w >= 8) {
    const int j = w - 8;
    const unsigned vo = j * 1024 + lane * 16;
    const char* src = kvg;
    unsigned dst = sbase + j * 1024;
#pragma unroll 1
    for (int i = 0; i < 5; ++i) { glds16(src, vo, dst); src += 4096; dst += 4096; }
    if (j == 0 && lane < 8) xflag[lane] = 0u;
    wait_vm<0>();
    bar();
#pragma unroll 1
    for (int i = 5; i < 15; ++i) { glds16(src, vo, dst); src += 4096; dst += 4096; }
    wait_vm<5>();
    __builtin_amdgcn_s_barrier();
#pragma unroll 1
    for (int i = 15; i < 39; ++i) { glds16(src, vo, dst); src += 4096; dst += 4096; }
    wait_vm<0>();
    __builtin_amdgcn_s_barrier();
    {
      auto rs = __builtin_amdgcn_make_buffer_rsrc(ws + WS_P5, 0, 256 * P5_SLOT, 0x00020000);
      const u32x4 sent = {0xffffffffu, 0xffffffffu, 0xffffffffu, 0xffffffffu};
#pragma unroll 1
      for (int i = 0; i < 9; ++i) __builtin_amdgcn_raw_buffer_store_b128(sent, rs, b * P5_SLOT + i * 4096 + vo, 0, 16);
    }
    const unsigned scr = sbase + 163584;
    {
      const char* base = (const char*)w2 + (size_t)((xg >> 1) * 1152 + idx * 36) * (S_ * 2) + (xg & 1) * 2048;
#pragma unroll 1
      for (int q = j; q < W2_K2; q += 4) touch(base, (q * 4 + (lane >> 4)) * (S_ * 2) + (lane & 15) * 128, scr);
      const char* base1 = (const char*)w1 + (size_t)(idx * 64) * (F_ * 2) + ((xg * 27) / 6) * 256;
#pragma unroll 1
      for (int q = j; q < 10; q += 4) {
        const int li = q * 64 + lane, rr = li / 10, c = li - rr * 10;
        touch(base1, rr * (F_ * 2) + c * 128, scr);
      }
      if (j < 3) { const int li = j * 64 + lane; touch((const char*)wo + idx * (162 * 128), (li < 162 ? li : 0) * 128, scr); }
    }
    wait_vm<0>();
    return;
  }
  const int qp = w & 3, kh = w >> 2;
  const int qt = qblk * 8 + qp * 2;
  u32x4 qa_u[2]; u32x2 qb_u[2]; unsigned km_u;
  gload16(qa_u[0], ws + WS_QA + (((size_t)head * 64 + qt) * 64 + lane) * 16);
  gload16(qa_u[1], ws + WS_QA + (((size_t)head * 64 + qt + 1) * 64 + lane) * 16);
  gload8(qb_u[0], ws + WS_QB + (((size_t)head * 64 + qt) * 32 + r) * 8);
  gload8(qb_u[1], ws + WS_QB + (((size_t)head * 64 + qt + 1) * 32 + r) * 8);
  gload4(km_u, ws + WS_KMAX + (head * 48 + (lane < 48 ? lane : 0)) * 4);
  asm volatile("s_waitcnt vmcnt(0)" : "+v"(qa_u[0]), "+v"(qa_u[1]), "+v"(qb_u[0]), "+v"(qb_u[1]), "+v"(km_u)::"memory");
  float km = __uint_as_float(km_u);
#pragma unroll
  for (int o = 1; o < 16; o <<= 1) km = fmaxf(km, __shfl_xor(km, o));
  const float kn2 = __builtin_bit_cast(float, __builtin_amdgcn_readlane(__builtin_bit_cast(int, km), 0)) +
                    __builtin_bit_cast(float, __builtin_amdgcn_readlane(__builtin_bit_cast(int, km), 16)) +
                    __builtin_bit_cast(float, __builtin_amdgcn_readlane(__builtin_bit_cast(int, km), 32));
  bf16x8 qa[2]; bf16x4 qb[2]; unsigned qlo[2];
#pragma unroll
  for (int a = 0; a < 2; ++a) {
    qlo[a] = hh == 0 ? qb_u[a][0] : 0u;
    float qn2 = dot2(qa_u[a][0], qa_u[a][0], dot2(qa_u[a][1], qa_u[a][1], dot2(qa_u[a][2], qa_u[a][2], dot2(qa_u[a][3], qa_u[a][3], dot2(qlo[a], qlo[a], 0.f)))));
    qn2 += __shfl_xor(qn2, 32);
    const float mb = __builtin_amdgcn_sqrtf(qn2 * kn2);
    u32x2 t;
    t[0] = qlo[a];
    t[1] = hh == 0 ? pack2(-mb, 0.f) : 0u;
    qa[a] = __builtin_bit_cast(bf16x8, qa_u[a]);
    qb[a] = __builtin_bit_cast(bf16x4, t);
  }
  const int dsel = r < 19 ? r : ((r >= 20 && r < 28) ? 0 : 4);
  const unsigned ka_p = sbase + lane * 16;
  const unsigned kb_p = sbase + 4096 + r * 8;
  const unsigned v_p = sbase + 5120 + (hh * 19 + dsel) * 16;
  f32x16 oa, ob;
#pragma unroll
  for (int i = 0; i < 16; ++i) { oa[i] = 0.f; ob[i] = 0.f; }
  const f32x16 zero16 = oa;
  PROF(1);
  rawbar();
  PROF(2);
  f32x16 sa0, sb0, sa1, sb1;
  bf16x8 kaN, v1p; bf16x4 kbN; u32x4 p1p = {0, 0, 0, 0};
  unsigned go = kh * 9984u;
  {
    bf16x8 ka0 = ldsread16(ka_p + go); bf16x4 kb0 = ldsread8(kb_p + go);
    kaN = ldsread16(ka_p + go + 1024); kbN = ldsread8(kb_p + go + 256);
    v1p = ldsread16(v_p);
    sa0 = MFMA32(ka0, qa[0], zero16); sb0 = MFMA32(ka0, qa[1], zero16);
    sa0 = MFMA32K8(kb0, qb[0], sa0); sb0 = MFMA32K8(kb0, qb[1], sb0);
  }
#define K2_UNIT(oP, v1P, oC, sC, sN, Q)                                                                \
    SBAR();                                                                                            \
    oP = MFMA32(v1P, __builtin_bit_cast(bf16x8, p1p), oP);                                             \
    SBAR();                                                                                            \
    p0[0] = exp2pk(sC[0], sC[1]); p0[1] = exp2pk(sC[2], sC[3]);                                        \
    SBAR();                                                                                            \
    sN = MFMA32(kaN, qa[Q], zero16);                                                                   \
    SBAR();                                                                                            \
    p0[2] = exp2pk(sC[4], sC[5]); p0[3] = exp2pk(sC[6], sC[7]);                                        \
    SBAR();                                                                                            \
    sN = MFMA32K8(kbN, qb[Q], sN);                                                                     \
    SBAR();                                                                                            \
    p1p[0] = exp2pk(sC[8], sC[9]); p1p[1] = exp2pk(sC[10], sC[11]);                                    \
    SBAR();                                                                                            \
    oC = MFMA32(v0, __builtin_bit_cast(bf16x8, p0), oC);                                               \
    SBAR();                                                                                            \
    p1p[2] = exp2pk(sC[12], sC[13]); p1p[3] = exp2pk(sC[14], sC[15]);                                  \
    SBAR();
#define K2_TILE(saC, sbC, saN, sbN, KOFF_A, KOFF_B, VOFF)                                              \
  {                                                                                                    \
    bf16x8 v0 = ldsread16(v_p + go + (VOFF)), v1 = ldsread16(v_p + go + (VOFF) + 608);                 \
    bf16x8 ka2 = ldsread16(ka_p + go + (KOFF_A)); bf16x4 kb2 = ldsread8(kb_p + go + (KOFF_B));         \
    u32x4 p0;                                                                                          \
    K2_UNIT(ob, v1p, oa, saC, saN, 0)                                                                  \
    K2_UNIT(oa, v1, ob, sbC, sbN, 1)                                                                   \
    v1p = v1; kaN = ka2; kbN = kb2;                                                                    \
  }
#define K2_QUAD                                                                                        \
  K2_TILE(sa0, sb0, sa1, sb1, 19968, 19968, 2432)                                                      \
  K2_TILE(sa1, sb1, sa0, sb0, 19968 + 1024, 19968 + 256, 3648)                                         \
  K2_TILE(sa0, sb0, sa1, sb1, 19968 + 2048, 19968 + 512, 19968)                                        \
  K2_TILE(sa1, sb1, sa0, sb0, 19968 + 3072, 19968 + 768, 19968 + 1216)
  K2_TILE(sa0, sb0, sa1, sb1, 2048, 512, 0)
  K2_TILE(sa1, sb1, sa0, sb0, 3072, 768, 1216)
  rawbar();
  K2_QUAD
  bar(); PROF(3);
#pragma unroll 1
  for (int it = 0; it < 6; ++it) { go += 19968u; K2_QUAD }
  go += 19968u;
  K2_TILE(sa0, sb0, sa1, sb1, 0, 0, 2432)
  K2_TILE(sa1, sb1, sa0, sb0, 1024, 256, 3648)
#undef K2_QUAD
#undef K2_TILE
#undef K2_UNIT
  ob = MFMA32(v1p, __builtin_bit_cast(bf16x8, p1p), ob);
  PROF(4);
  f32x16 of, og;
  if (kh) { of = ob; og = oa; } else { of = oa; og = ob; }
  {
    f32x4* xch = (f32x4*)(smem + w * 2560) + lane;
    xch[0] = f32x4{og[0], og[1], og[2], og[3]}; xch[64] = f32x4{og[4], og[5], og[6], og[7]};
    if (hh == 0) xch[128] = f32x4{og[8], og[9], og[10], og[11]};
    asm volatile("s_waitcnt lgkmcnt(0)" ::: "memory");
    xflag[w] = 1u;
    while (__builtin_amdgcn_readfirstlane(xflag[w ^ 4]) == 0u) __builtin_amdgcn_s_sleep(1);
    asm volatile("" ::: "memory");
    const f32x4* pch = (const f32x4*)(smem + (w ^ 4) * 2560) + lane;
    const f32x4 x0 = pch[0], x1 = pch[64], x2 = pch[hh == 0 ? 128 : 0];
#pragma unroll
    for (int i = 0; i < 4; ++i) { of[i] += x0[i]; of[4 + i] += x1[i]; of[8 + i] += x2[i]; }
  }
  float l = __shfl(of[10], r);
  if (__builtin_expect(__any(!(l >= 5.421e-20f)), 0)) {
    u32x2 qz; qz[0] = kh ? qlo[1] : qlo[0]; qz[1] = 0u;
    k2_exact(kvg, lane, kh ? qa[1] : qa[0], __builtin_bit_cast(bf16x4, qz), of);
    l = __shfl(of[10], r);
  }
  const float rl = 1.0f / l;
  {
    const int q = (qt + kh) * 32 + r;
    char* obase = ws + WS_O + (size_t)(q >> 4) * 18432 + (q & 15) * 16;
    auto st2 = [&](int d, float a0, float a1) {
      const int col = head * DH_ + d;
      *(unsigned*)(obase + (col >> 3) * 256 + (col & 7) * 2) = pack2(a0 * rl, a1 * rl);
    };
    st2(4 * hh, of[0], of[1]); st2(4 * hh + 2, of[2], of[3]);
    st2(8 + 4 * hh, of[4], of[5]); st2(8 + 4 * hh + 2, of[6], of[7]);
    if (hh == 0) st2(16, of[8], of[9]);
  }
  PROF(5); PROF_FLUSH(2);
}

#define K3_DEPTH 3
struct C3 { static constexpr int NST = 9, NSLOT = 9, DEPTH = K3_DEPTH, ABYTES = 8192, BRS = 144, SLOT = ABYTES + 64 * BRS, AMODE = 0, NRT = 4;
            static constexpr int LA = 2, LASTA = 64, LB = 3, LASTB = 16, SA = 2048, SB = 64 * D_ * 2;
            static constexpr bool SWAP = true; };
template <int NC>
DI void k3_epilogue(char* smem, unsigned sbase, int lane, int w, int wm, int wn, int rb, int ct, f32x4 (&acc)[1][3],
                    const u32x2 (&xr)[3], char* ws PROF_ARGS) {
  const int r = lane & 15, g = lane >> 4, q4 = (lane & 15) >> 2, p4 = lane & 3;
  const int token = rb * 64 + wm * 16 + r;
  const int c0 = wn * 3, win0 = ct * 72;
  float hv[NC][4];
  u32x2 hp[NC];
  float sum = 0.f;
#pragma unroll
  for (int c = 0; c < NC; ++c) {
    const bool valid = (c0 + c) * 16 + 4 * g < 72;
    hp[c][0] = pack2(bflo(xr[c][0]) + acc[0][c][0], bfhi(xr[c][0]) + acc[0][c][1]);
    hp[c][1] = pack2(bflo(xr[c][1]) + acc[0][c][2], bfhi(xr[c][1]) + acc[0][c][3]);
    hv[c][0] = bflo(hp[c][0]); hv[c][1] = bfhi(hp[c][0]); hv[c][2] = bflo(hp[c][1]); hv[c][3] = bfhi(hp[c][1]);
    if (valid) sum += (hv[c][0] + hv[c][1]) + (hv[c][2] + hv[c][3]);
  }
  sum += __shfl_xor(sum, 16); sum += __shfl_xor(sum, 32);
  const float lm = sum * (NC == 3 ? 1.0f / 48 : 1.0f / 24);
  float m2 = 0.f;
#pragma unroll
  for (int c = 0; c < NC; ++c) {
    if ((c0 + c) * 16 + 4 * g < 72) {
#pragma unroll
      for (int i = 0; i < 4; ++i) { float d = hv[c][i] - lm; m2 += d * d; }
    }
  }
  m2 += __shfl_xor(m2, 16); m2 += __shfl_xor(m2, 32);
  unsigned long long* st = (unsigned long long*)(ws + WS_STAT) + (size_t)rb * 1024;
  {
    unsigned long long v = ((unsigned long long)__float_as_uint(m2) << 32) | __float_as_uint(lm);
    if (v == STAT_SENT) v = 0x7fc000007fc00000ull;
    if (g == 0) __hip_atomic_store(st + (ct * 2 + wn) * 64 + wm * 16 + r, v, __ATOMIC_RELAXED, __HIP_MEMORY_SCOPE_AGENT);
  }
  {
    unsigned short* hrow = (unsigned short*)(ws + WS_H) + (size_t)token * D_ + win0 + 4 * g;
#pragma unroll
    for (int c = 0; c < NC; ++c)
      if ((c0 + c) * 16 + 4 * g < 72) *(u32x2*)(hrow + (c0 + c) * 16) = hp[c];
  }
  PROF(3);
  unsigned long long sv[4];
  {
    const unsigned long long* p0 = st + (4 * g) * 64 + wm * 16 + r;
    for (unsigned spin = 0; spin < (1u << 24); ++spin) {
      bool ok = true;
#pragma unroll
      for (int k = 0; k < 4; ++k) { sv[k] = __hip_atomic_load(p0 + k * 64, __ATOMIC_RELAXED, __HIP_MEMORY_SCOPE_AGENT); ok &= sv[k] != STAT_SENT; }
      if (__all(ok)) break;
      __builtin_amdgcn_s_sleep(1);
    }
  }
  PROF(4);
  float msum = 0.f, mk[4];
#pragma unroll
  for (int k = 0; k < 4; ++k) { mk[k] = __uint_as_float((unsigned)sv[k]); msum += ((k & 1) ? 24.f : 48.f) * mk[k]; }
  msum += __shfl_xor(msum, 16); msum += __shfl_xor(msum, 32);
  const float mean = msum * (1.0f / D_);
  float dv = 0.f;
#pragma unroll
  for (int k = 0; k < 4; ++k) { float d = mk[k] - mean; dv += __uint_as_float((unsigned)(sv[k] >> 32)) + ((k & 1) ? 24.f : 48.f) * d * d; }
  dv += __shfl_xor(dv, 16); dv += __shfl_xor(dv, 32);
  const float var = dv * (1.0f / D_);
  const float r1 = __builtin_amdgcn_rsqf(var + 1e-5f);
  const float r2 = __builtin_amdgcn_rsqf(var * r1 * r1 + 1e-5f);
  const float rr = r1 * r2;
  constexpr int TRS = 168;
#pragma unroll
  for (int c = 0; c < NC; ++c) {
    u32x2 pk = {pack2((hv[c][0] - mean) * rr, (hv[c][1] - mean) * rr), pack2((hv[c][2] - mean) * rr, (hv[c][3] - mean) * rr)};
    *(u32x2*)(smem + (wm * 16 + r) * TRS + ((c0 + c) * 16 + 4 * g) * 2) = pk;
  }
  bar();
  for (int it = w; it < 10; it += 8) {
    const int kk = it / 5, c = it - kk * 5;
    unsigned ad = sbase + (kk * 32 + 8 * g + q4) * TRS + (c * 16 + 4 * p4) * 2;
    bf16x8 v = cat8(trread(ad), trread(ad + 4 * TRS));
    const int col = c * 16 + r;
    if (col < 72) {
      const int d = win0 + col;
      const int mt = d / 96, dd = d - mt * 96;
      const int k32 = rb * 2 + kk;
      char* dst = ws + WS_HNT + ((((size_t)mt * 64 + k32) * 6 + (dd >> 4)) * 64 + g * 16 + (dd & 15)) * 16;
      *(u32x4*)dst = __builtin_bit_cast(u32x4, v);
    }
  }
}

__global__ __launch_bounds__(768) void k3_oproj(const unsigned short* __restrict__ x, const unsigned short* __restrict__ wo,
                                                char* __restrict__ ws) {
  __shared__ __attribute__((aligned(16))) char smem[C3::NSLOT * C3::SLOT + 256];
  const int b = blockIdx.x, xg = b & 7, idx = b >> 3;
  const int rb = xg * 4 + (idx >> 3), ct = idx & 7;
  const int tid = threadIdx.x, lane = tid & 63;
  const int w = __builtin_amdgcn_readfirstlane(tid >> 6);
  const int r = lane & 15, g = lane >> 4;
  const unsigned sbase = lds_addr(smem);
  PROF_DECL PROF(0);
  if (w >= 8) {
    const int j = w - 8;
    unsigned oa[2], ob[3];
#pragma unroll
    for (int i = 0; i < 2; ++i) oa[i] = (j * 18 + i) * 1024 + lane * 16;
#pragma unroll
    for (int i = 0; i < 3; ++i) {
      const int c = i < 2 ? (i * 4 + j) * 64 + lane : 512 + j * 16 + (lane & 15);
      const int rho = c / 9, cc = c - rho * 9;
      ob[i] = swap23(rho) * (D_ * 2) + cc * 16;
    }
    PROF(1);
    gemm_loader<C3>(sbase, j, lane, ws + WS_O + (size_t)rb * (4 * 18432), (const char*)(wo + ct * 72), oa, ob);
    PROF(2); PROF_FLUSH(3);
    return;
  }
  const int wm = w & 3, wn = w >> 2;
  u32x2 xr[3];
  {
    const char* xrow = (const char*)(x + (size_t)(rb * 64 + wm * 16 + r) * D_ + ct * 72 + wn * 48 + 4 * g);
    gload8(xr[0], xrow); gload8(xr[1], xrow + ((wn == 1 && g >= 2) ? 0 : 32)); gload8(xr[2], xrow + (wn == 0 ? 64 : 0));
  }
  const int q4 = (lane & 15) >> 2, p4 = lane & 3;
  const unsigned bbase = sbase + C3::ABYTES + (16 * (g >> 1) + 4 * (g & 1) + q4) * C3::BRS + p4 * 8 + wn * 96;
  f32x4 acc[1][3], st[5];
#pragma unroll
  for (int j = 0; j < 3; ++j) acc[0][j] = f32x4{0, 0, 0, 0};
  if (wn == 0) {
    unsigned bb[3] = {bbase, bbase + 32, bbase + 64};
    gemm_core<C3, 1, 3, 0>(sbase, lane, wm, bb, acc, st);
    asm volatile("s_waitcnt vmcnt(0)" : "+v"(xr[0]), "+v"(xr[1]), "+v"(xr[2])::"memory");
    PROF(1);
    k3_epilogue<3>(smem, sbase, lane, w, wm, wn, rb, ct, acc, xr, ws PROF_PASS);
  } else {
    unsigned bb[2] = {bbase, bbase + 32};
    gemm_core<C3, 1, 2, 0>(sbase, lane, wm, bb, acc, st);
    asm volatile("s_waitcnt vmcnt(0)" : "+v"(xr[0]), "+v"(xr[1]), "+v"(xr[2])::"memory");
    PROF(1);
    k3_epilogue<2>(smem, sbase, lane, w, wm, wn, rb, ct, acc, xr, ws PROF_PASS);
  }
  PROF(2); PROF_FLUSH(3);
}

#define K4_DEPTH 3
struct C4 { static constexpr int NST = 32, NSLOT = 5, DEPTH = K4_DEPTH, ABYTES = 12288, BRS = 256, SLOT = ABYTES + 64 * BRS, AMODE = 0, NRT = 6;
            static constexpr int LA = 3, LASTA = 64, LB = 4, LASTB = 64, SA = 12288, SB = 64 * F_ * 2;
            static constexpr bool SWAP = true; };
DI float gelu_tanh(float x) {
  float u = x * (0.7978845608028654f + 0.0356774081363001f * x * x);
  float e = __builtin_amdgcn_exp2f(-2.8853900817779268f * u);
  return x * __builtin_amdgcn_rcpf(1.0f + e);
}
__global__ __launch_bounds__(768) void k4_gemm1(const unsigned short* __restrict__ w1, const unsigned short* __restrict__ b1,
                                                const unsigned short* __restrict__ w2, char* __restrict__ ws) {
  __shared__ __attribute__((aligned(16))) char smem[C4::NSLOT * C4::SLOT];
  const int b = blockIdx.x, xg = b & 7, idx = b >> 3;
  const int tid = threadIdx.x, lane = tid & 63;
  const int w = __builtin_amdgcn_readfirstlane(tid >> 6);
  const unsigned sbase = lds_addr(smem);
  PROF_DECL PROF(0);
  if (idx >= 27) {
    const int sw = (idx - 27) * 12 + w;
    constexpr int PER = 9 - W2_K2;
    const char* base = (const char*)w2 + (size_t)((xg >> 1) * 1152) * (S_ * 2) + (xg & 1) * 2048;
#pragma unroll 1
    for (int q = sw; q < 32 * PER; q += 60) {
      const int ch = q / (PER > 0 ? PER : 1);
      touch(base, (ch * 36 + (W2_K2 + q - ch * PER) * 4 + (lane >> 4)) * (S_ * 2) + (lane & 15) * 128, sbase);
    }
    wait_vm<0>();
    return;
  }
  const int t = xg * 27 + idx;
  const int ntile = t / 6, mt = t - ntile * 6;
  const int r = lane & 15, g = lane >> 4;
  if (w >= 8) {
    const int j = w - 8;
    unsigned oa[3], ob[4];
#pragma unroll
    for (int i = 0; i < 3; ++i) oa[i] = (i * 4 + j) * 1024 + lane * 16;
#pragma unroll
    for (int i = 0; i < 4; ++i) {
      const int rho = (i * 4 + j) * 4 + (lane >> 4), cc = (lane & 15) ^ ((rho & 7) << 1);
      ob[i] = swap23(rho) * (F_ * 2) + cc * 16;
    }
    gemm_loader<C4>(sbase, j, lane, ws + WS_HNT + (size_t)mt * (64 * 6144), (const char*)(w1 + ntile * 128), oa, ob);
    return;
  }
  const int wm = w >> 2, wn = w & 3;
  u32x2 bias[2];
  gload8(bias[0], b1 + ntile * 128 + wn * 32 + 4 * g); gload8(bias[1], b1 + ntile * 128 + wn * 32 + 16 + 4 * g);
  const int q4 = (lane & 15) >> 2, p4 = lane & 3;
  const int rho = 16 * (g >> 1) + 4 * (g & 1) + q4;
  unsigned bb[2];
#pragma unroll
  for (int j = 0; j < 2; ++j) bb[j] = sbase + C4::ABYTES + rho * 256 + (((wn * 2 + j) ^ (rho & 7)) * 32) + p4 * 8;
  f32x4 acc[3][2], st[5];
#pragma unroll
  for (int i = 0; i < 3; ++i)
#pragma unroll
    for (int j = 0; j < 2; ++j) acc[i][j] = f32x4{0, 0, 0, 0};
  PROF(1);
  gemm_core<C4, 3, 2, 0>(sbase, lane, wm * 3, bb, acc, st);
  asm volatile("s_waitcnt vmcnt(0)" : "+v"(bias[0]), "+v"(bias[1])::"memory");
  PROF(2);
#pragma unroll
  for (int i = 0; i < 3; ++i) {
    const int d = mt * 96 + (wm * 3 + i) * 16 + r;
    const int mb = d / 144, dd = d - mb * 144;
    char* dst = ws + WS_U + ((((size_t)mb * 144 + ntile * 4 + wn) * 9 + (dd >> 4)) * 64 + (g >> 1) * 16 + (dd & 15)) * 16 + (g & 1) * 8;
#pragma unroll
    for (int j = 0; j < 2; ++j) {
      const f32x4 v = acc[i][j];
      float y0 = gelu_tanh(v[0] + bflo(bias[j][0])), y1 = gelu_tanh(v[1] + bfhi(bias[j][0]));
      float y2 = gelu_tanh(v[2] + bflo(bias[j][1])), y3 = gelu_tanh(v[3] + bfhi(bias[j][1]));
      *(u32x2*)(dst + j * 512) = u32x2{pack2(y0, y1), pack2(y2, y3)};
    }
  }
  PROF(3); PROF_FLUSH(4);
}

#define K5_DEPTH 2
struct C5 { static constexpr int NST = 18, NSLOT = 4, DEPTH = K5_DEPTH, ABYTES = 18432, BRS = 256, SLOT = ABYTES + 64 * BRS, AMODE = 0, NRT = 9;
            static constexpr int LA = 5, LASTA = 32, LB = 4, LASTB = 64, SA = 18432, SB = 64 * S_ * 2;
            static constexpr bool SWAP = false; };
DI unsigned pack2s(float lo, float hi) { const unsigned v = pack2(lo, hi); return v == 0xffffffffu ? 0x7fc07fc0u : v; }
template <int NR>
DI void k5_epilogue(int tile, int kq, int w, int lane, int mblk, int ntile, int rt0, int ct0, f32x4 (&acc)[5][2], const unsigned short* b2,
                    char* ws, unsigned short* out PROF_ARGS) {
  const int r = lane & 15, g = lane >> 4;
  auto rs = __builtin_amdgcn_make_buffer_rsrc(ws + WS_P5, 0, 256 * P5_SLOT, 0x00020000);
  constexpr int HALF = (NR + 1) / 2;
#pragma unroll
  for (int o = 0; o < 4; ++o) {
    if (o != kq) {
      const int j = o >> 1, i0 = (o & 1) ? HALF : 0, cnt = (o & 1) ? NR - HALF : HALF;
      const int base = (tile * 4 + o) * P5_SLOT + (w * 3 + ((kq - o - 1) & 3)) * 1536;
      u32x4 v = {pack2s(acc[i0][j][0], acc[i0][j][1]), pack2s(acc[i0][j][2], acc[i0][j][3]),
                 pack2s(acc[i0 + 1][j][0], acc[i0 + 1][j][1]), pack2s(acc[i0 + 1][j][2], acc[i0 + 1][j][3])};
      __builtin_amdgcn_raw_buffer_store_b128(v, rs, base + lane * 16, 0, 16);
      if (cnt == 3) {
        u32x2 v2 = {pack2s(acc[i0 + 2][j][0], acc[i0 + 2][j][1]), pack2s(acc[i0 + 2][j][2], acc[i0 + 2][j][3])};
        __builtin_amdgcn_raw_buffer_store_b64(v2, rs, base + 1024 + lane * 8, 0, 16);
      }
    }
  }
  const int jm = kq >> 1, i0 = (kq & 1) ? HALF : 0, cnt = (kq & 1) ? NR - HALF : HALF;
  const unsigned short* h = (const unsigned short*)(ws + WS_H);
  const int s = ntile * 128 + (ct0 + jm) * 16 + r;
  u32x2 hres[3];
#pragma unroll
  for (int k = 0; k < 3; ++k) {
    const int i = i0 + (k < cnt ? k : 0);
    hres[k] = *(const u32x2*)(h + (size_t)s * D_ + mblk * 144 + (rt0 + i) * 16 + 4 * g);
  }
  const float bias = __uint_as_float(((unsigned)b2[s]) << 16);
  f32x4 mine[3];
  if (kq == 0) { mine[0] = acc[0][0]; mine[1] = acc[1][0]; mine[2] = acc[2][0]; }
  else if (kq == 1) { mine[0] = acc[HALF][0]; mine[1] = acc[HALF + 1][0]; mine[2] = acc[0][0]; }
  else if (kq == 2) { mine[0] = acc[0][1]; mine[1] = acc[1][1]; mine[2] = acc[2][1]; }
  else { mine[0] = acc[HALF][1]; mine[1] = acc[HALF + 1][1]; mine[2] = acc[0][1]; }
  PROF(4);
  u32x4 pa[3]; u32x2 pb[3];
  const int inbox = (tile * 4 + kq) * P5_SLOT + w * 3 * 1536;
  for (unsigned spin = 0; spin < (1u << 22); ++spin) {
    unsigned all = 0u;
#pragma unroll
    for (int p = 0; p < 3; ++p) {
      pa[p] = __builtin_amdgcn_raw_buffer_load_b128(rs, inbox + p * 1536 + lane * 16, 0, 16);
      pb[p] = u32x2{0u, 0u};
      if (cnt == 3) pb[p] = __builtin_amdgcn_raw_buffer_load_b64(rs, inbox + p * 1536 + 1024 + lane * 8, 0, 16);
    }
#pragma unroll
    for (int p = 0; p < 3; ++p)
      all |= (pa[p][0] == 0xffffffffu) | (pa[p][1] == 0xffffffffu) | (pa[p][2] == 0xffffffffu) | (pa[p][3] == 0xffffffffu) |
             (pb[p][0] == 0xffffffffu) | (pb[p][1] == 0xffffffffu);
    if (!__any(all != 0u)) break;
    asm volatile("" ::: "memory");
    __builtin_amdgcn_s_sleep(2);
  }
  PROF(5);
#pragma unroll
  for (int p = 0; p < 3; ++p) {
    mine[0] += f32x4{bflo(pa[p][0]), bfhi(pa[p][0]), bflo(pa[p][1]), bfhi(pa[p][1])};
    mine[1] += f32x4{bflo(pa[p][2]), bfhi(pa[p][2]), bflo(pa[p][3]), bfhi(pa[p][3])};
    mine[2] += f32x4{bflo(pb[p][0]), bfhi(pb[p][0]), bflo(pb[p][1]), bfhi(pb[p][1])};
  }
#pragma unroll
  for (int k = 0; k < 3; ++k) {
    if (k < cnt) {
      const f32x4 v = mine[k];
      const int d = mblk * 144 + (rt0 + i0 + k) * 16 + 4 * g;
      u32x2 o = {pack2(bflo(hres[k][0]) + (v[0] + bias), bfhi(hres[k][0]) + (v[1] + bias)),
                 pack2(bflo(hres[k][1]) + (v[2] + bias), bfhi(hres[k][1]) + (v[3] + bias))};
      *(u32x2*)(out + (size_t)s * D_ + d) = o;
    }
  }
}

__global__ __launch_bounds__(768) void k5_gemm2(const unsigned short* __restrict__ w2, const unsigned short* __restrict__ b2,
                                                char* __restrict__ ws, unsigned short* __restrict__ out) {
  __shared__ __attribute__((aligned(16))) char smem[C5::NSLOT * C5::SLOT];
  const int b = blockIdx.x, xg = b & 7, idx = b >> 3;
  const int kq = xg >> 1, ntile = (xg & 1) * 8 + (idx & 7), mblk = idx >> 3;
  const int tile = mblk * 16 + ntile;
  const int tid = threadIdx.x, lane = tid & 63;
  const int w = __builtin_amdgcn_readfirstlane(tid >> 6);
  const unsigned sbase = lds_addr(smem);
  const int g = lane >> 4;
  PROF_DECL PROF(0);
  if (w >= 8) {
    const int j = w - 8;
    unsigned oa[5], ob[4];
#pragma unroll
    for (int i = 0; i < 4; ++i) oa[i] = (i * 4 + j) * 1024 + lane * 16;
    oa[4] = 16384 + j * 512 + (lane & 31) * 16;
#pragma unroll
    for (int i = 0; i < 4; ++i) {
      const int rho = (i * 4 + j) * 4 + (lane >> 4), cc = (lane & 15) ^ ((rho & 7) << 1);
      ob[i] = swap23(rho) * (S_ * 2) + cc * 16;
    }
    gemm_loader<C5>(sbase, j, lane, ws + WS_U + ((size_t)mblk * 144 + kq * 36) * 9216, (const char*)(w2 + (size_t)(kq * 1152) * S_ + ntile * 128), oa, ob);
    return;
  }
  const int wr = w >> 2, wc = w & 3;
  const int q4 = (lane & 15) >> 2, p4 = lane & 3;
  const int rho = 16 * (g >> 1) + 4 * (g & 1) + q4;
  unsigned bb[2];
#pragma unroll
  for (int j = 0; j < 2; ++j) bb[j] = sbase + C5::ABYTES + rho * 256 + (((wc * 2 + j) ^ (rho & 7)) * 32) + p4 * 8;
  f32x4 acc[5][2], st[5];
#pragma unroll
  for (int i = 0; i < 5; ++i)
#pragma unroll
    for (int j = 0; j < 2; ++j) acc[i][j] = f32x4{0, 0, 0, 0};
  PROF(1);
  if (wr == 0) { gemm_core<C5, 5, 2, 0>(sbase, lane, 0, bb, acc, st); PROF(2); k5_epilogue<5>(tile, kq, w, lane, mblk, ntile, 0, 2 * wc, acc, b2, ws, out PROF_PASS); }
  else { gemm_core<C5, 4, 2, 0>(sbase, lane, 5, bb, acc, st); PROF(2); k5_epilogue<4>(tile, kq, w, lane, mblk, ntile, 5, 2 * wc, acc, b2, ws, out PROF_PASS); }
  PROF(3); PROF_FLUSH(5);
}

extern "C" void kernel_launch(void* const* d_in, const int* in_sizes, int n_in, void* d_out, int out_size, void* d_ws, size_t ws_size,
                              hipStream_t stream) {
  const unsigned short* x = (const unsigned short*)d_in[0];
  const unsigned short* wqkv = (const unsigned short*)d_in[1];
  const unsigned short* wo = (const unsigned short*)d_in[2];
  const unsigned short* w1 = (const unsigned short*)d_in[4];
  const unsigned short* b1 = (const unsigned short*)d_in[5];
  const unsigned short* w2 = (const unsigned short*)d_in[6];
  const unsigned short* b2 = (const unsigned short*)d_in[7];
  char* ws = (char*)d_ws;
  k1_qkv<<<256, 768, 0, stream>>>(x, wqkv, ws);
  k2_attn<<<256, 768, 0, stream>>>(ws, wo, w1, w2);
  k3_oproj<<<256, 768, 0, stream>>>(x, wo, ws);
  k4_gemm1<<<256, 768, 0, stream>>>(w1, b1, w2, ws);
  k5_gemm2<<<256, 768, 0, stream>>>(w2, b2, ws, (unsigned short*)d_out);
}
```

```cpp
#include <hip/hip_runtime.h>
#include <hip/hip_bf16.h>
#include <stdint.h>

typedef __attribute__((ext_vector_type(8))) short bf16x8;
typedef __attribute__((ext_vector_type(4))) short bf16x4;
typedef __attribute__((ext_vector_type(4))) float f32x4;
typedef __attribute__((ext_vector_type(2))) float f32x2;
typedef __attribute__((ext_vector_type(16))) float f32x16;
typedef __attribute__((ext_vector_type(4))) unsigned u32x4;
typedef __attribute__((ext_vector_type(2))) unsigned u32x2;
typedef __attribute__((ext_vector_type(2))) __bf16 bf2_t;
#define LDSP __attribute__((address_space(3)))
#define DI __device__ __forceinline__

constexpr int S_ = 2048, D_ = 576, H_ = 32, DH_ = 18, F_ = 4608;

constexpr size_t WS_QA = 0;
constexpr size_t WS_QB = WS_QA + 32u * 64 * 1024;
constexpr size_t KV_HEAD = 163840;
constexpr size_t WS_KV = WS_QB + 32u * 64 * 256;
constexpr size_t WS_KMAX = WS_KV + 32 * KV_HEAD;
constexpr size_t WS_O = WS_KMAX + 8192;
constexpr size_t WS_H = WS_O + (size_t)S_ * D_ * 2;
constexpr size_t WS_HNT = WS_H + (size_t)S_ * D_ * 2;
constexpr size_t WS_U = WS_HNT + (size_t)S_ * D_ * 2;
constexpr size_t WS_STAT = WS_U + (size_t)D_ * F_ * 2;
constexpr size_t WS_WOT = WS_STAT + 262144;
constexpr size_t WS_P5 = WS_WOT + (size_t)D_ * D_ * 2;
constexpr int P5_SLOT = 8 * 3 * 1536;
constexpr size_t WS_END = WS_P5 + 256u * P5_SLOT;

#define PROF_DECL
#define PROF(slot)
#define PROF_FLUSH(kid)
#define PROF_ARGS
#define PROF_PASS
constexpr unsigned long long STAT_SENT = 0xFFFFFFFFFFFFFFFFull;

DI unsigned pack2(float lo, float hi) {
  f32x2 f = {lo, hi};
  bf2_t h = __builtin_convertvector(f, bf2_t);
  return __builtin_bit_cast(unsigned, h);
}
DI float bflo(unsigned u) { return __uint_as_float(u << 16); }
DI float bfhi(unsigned u) { return __uint_as_float(u & 0xffff0000u); }
DI float dot2(unsigned a, unsigned b, float c) {
  return __builtin_amdgcn_fdot2_f32_bf16(__builtin_bit_cast(bf2_t, a), __builtin_bit_cast(bf2_t, b), c, false);
}
DI bf16x4 trread(unsigned addr) { return __builtin_amdgcn_ds_read_tr16_b64_v4i16((LDSP bf16x4*)(uintptr_t)addr); }
DI bf16x8 ldsread16(unsigned addr) { return *(const LDSP bf16x8*)(uintptr_t)addr; }
DI bf16x4 ldsread8(unsigned addr) { return *(const LDSP bf16x4*)(uintptr_t)addr; }
DI bf16x8 cat8(bf16x4 a, bf16x4 b) { return __builtin_shufflevector(a, b, 0, 1, 2, 3, 4, 5, 6, 7); }
DI unsigned lds_addr(const void* p) { return (unsigned)(uintptr_t)(LDSP const char*)p; }
#define MFMA16(a, b, c) __builtin_amdgcn_mfma_f32_16x16x32_bf16((a), (b), (c), 0, 0, 0)
#define MFMA32(a, b, c) __builtin_amdgcn_mfma_f32_32x32x16_bf16((a), (b), (c), 0, 0, 0)
#define MFMA32K8(a, b, c) __builtin_amdgcn_mfma_f32_32x32x8bf16_1k((a), (b), (c), 0, 0, 0)
#define SBAR() __builtin_amdgcn_sched_barrier(0)
#define SENT_K2 0
#define K2_SCHED 0
#define K2_PHI 0
#define G_KS 0
#define K1_ROT 1
#define K3_ROT 1
#define K4_SEG 16
#define K5_SEG 18

template <int N> DI void wait_vm() { asm volatile("s_waitcnt vmcnt(%0)" ::"i"(N) : "memory"); }
DI void bar() { asm volatile("" ::: "memory"); __builtin_amdgcn_s_waitcnt(0xC07F); __builtin_amdgcn_s_barrier(); asm volatile("" ::: "memory"); }
DI void rawbar() { asm volatile("" ::: "memory"); __builtin_amdgcn_s_barrier(); asm volatile("" ::: "memory"); }

DI void glds16(const char* ubase, unsigned voff, unsigned lds_dst) {
  asm volatile("s_mov_b32 m0, %2\n\ts_nop 0\n\tglobal_load_lds_dwordx4 %0, %1" ::"v"(voff), "s"(ubase), "s"(__builtin_amdgcn_readfirstlane(lds_dst)) : "memory");
}
DI void glds16nt(const char* ubase, unsigned voff, unsigned lds_dst) {
  asm volatile("s_mov_b32 m0, %2\n\ts_nop 0\n\tglobal_load_lds_dwordx4 %0, %1 nt" ::"v"(voff), "s"(ubase), "s"(__builtin_amdgcn_readfirstlane(lds_dst)) : "memory");
}
DI void touch(const char* ubase, unsigned voff, unsigned lds_dst) {
  asm volatile("s_mov_b32 m0, %2\n\ts_nop 0\n\tglobal_load_lds_dword %0, %1" ::"v"(voff), "s"(ubase), "s"(__builtin_amdgcn_readfirstlane(lds_dst)) : "memory");
}
DI void touch_nt(const char* ubase, unsigned voff, unsigned lds_dst) {
  asm volatile("s_mov_b32 m0, %2\n\ts_nop 0\n\tglobal_load_lds_dword %0, %1 nt" ::"v"(voff), "s"(ubase), "s"(__builtin_amdgcn_readfirstlane(lds_dst)) : "memory");
}
DI void gload16(u32x4& dst, const void* gsrc) { dst = *(const u32x4*)gsrc; }
DI void gload8(u32x2& dst, const void* gsrc) { dst = *(const u32x2*)gsrc; }
DI void gload4(unsigned& dst, const void* gsrc) { dst = *(const unsigned*)gsrc; }
DI int swap23(int r) { return (r & ~12) | ((r & 4) << 1) | ((r & 8) >> 1); }

template <class C>
DI void gemm_loader(unsigned sbase, int j, int lane, const char* abase, const char* bbase, const unsigned (&oa)[C::LA], const unsigned (&ob)[C::LB],
                    int s0) {
  constexpr int L = C::LA + C::LB, P = C::PART;
  static_assert(P >= 1 && P <= L && C::NSLOT >= 4 && C::NST >= 3, "");
  abase += (size_t)s0 * C::SA; bbase += (size_t)s0 * C::SB;
  int cur = s0;
  unsigned so = 0;
  auto issue = [&](int lo, int hi) {
#pragma unroll
    for (int q = 0; q < L; ++q) {
      if (q >= lo && q < hi) {
        const bool isA = C::ORD[q] < C::LA;
        const int i = isA ? C::ORD[q] : C::ORD[q] - C::LA;
        if (isA) {
          if (i == C::LA - 1 && C::LASTA < 64) { if (lane < C::LASTA) glds16(abase, oa[i], sbase + so + i * 4096 + j * (C::LASTA * 16)); }
          else glds16(abase, oa[i], sbase + so + i * 4096 + j * 1024);
        } else {
          if (i == C::LB - 1 && C::LASTB < 64) { if (lane < C::LASTB) glds16(bbase, ob[i], sbase + so + C::ABYTES + i * 4096 + j * (C::LASTB * 16)); }
          else glds16(bbase, ob[i], sbase + so + C::ABYTES + i * 4096 + j * 1024);
        }
      }
    }
  };
  auto next = [&]() {
    ++cur;
    const int adv = cur == C::NST ? 1 - C::NST : 1;
    cur = cur == C::NST ? 0 : cur;
    abase += (ptrdiff_t)adv * C::SA; bbase += (ptrdiff_t)adv * C::SB;
    so = so + C::SLOT == C::NSLOT * C::SLOT ? 0u : so + C::SLOT;
  };
  constexpr int DF = C::DF;
  static_assert(DF + 4 <= C::NSLOT && (DF + 2) * L <= 63, "");
#pragma unroll
  for (int d = 0; d <= DF; ++d) { issue(0, L); next(); }
  issue(0, P);
#pragma unroll 1
  for (int k = 0; k < C::NST - 2 - DF; ++k) {
    wait_vm<DF * L + P>();
    __builtin_amdgcn_s_barrier();
    issue(P, L); next();
    issue(0, P);
  }
  wait_vm<DF * L + P>();
  __builtin_amdgcn_s_barrier();
  issue(P, L);
  if (DF >= 2) { wait_vm<(DF >= 2 ? 2 : 0) * L>(); __builtin_amdgcn_s_barrier(); }
  if (DF >= 1) { wait_vm<(DF >= 1 ? 1 : 0) * L>(); __builtin_amdgcn_s_barrier(); }
  wait_vm<0>();
  __builtin_amdgcn_s_barrier();
}

template <class C, int NR, int NC, int STATS, int AR, int AC>
DI void gemm_core(unsigned sbase, int lane, int rt0, const unsigned (&bb)[NC], f32x4 (&acc)[AR][AC], f32x4 (&st)[5] PROF_ARGS) {
  const int r = lane & 15, g = lane >> 4;
  unsigned a0, a1;
  if (C::AMODE == 0) { a0 = sbase + rt0 * 1024 + lane * 16; a1 = a0 + C::NRT * 1024; }
  else { a0 = sbase + (rt0 * 16 + r) * 128 + ((g ^ (r & 7)) * 16); a1 = sbase + (rt0 * 16 + r) * 128 + (((4 + g) ^ (r & 7)) * 16); }
  constexpr int ASTEP = C::AMODE == 0 ? 1024 : 2048;
  const bf16x8 ones = {0x3f80, 0x3f80, 0x3f80, 0x3f80, 0x3f80, 0x3f80, 0x3f80, 0x3f80};
  bf16x8 fa0[NR], fb0[NC], fa1[NR], fb1[NC];
  auto readf = [&](bf16x8 (&fa)[NR], bf16x8 (&fb)[NC], unsigned so, int kk) {
    fa[0] = ldsread16((kk ? a1 : a0) + so);
#pragma unroll
    for (int j = 0; j < NC; ++j) {
      if (C::BMODE == 0) fb[j] = cat8(trread(bb[j] + so + kk * 32 * C::BRS), trread(bb[j] + so + kk * 32 * C::BRS + 8 * C::BRS));
      else fb[j] = ldsread16(bb[j] + so + kk * 1024);
    }
#pragma unroll
    for (int i = 1; i < NR; ++i) fa[i] = ldsread16((kk ? a1 : a0) + so + i * ASTEP);
  };
  auto mma = [&](bf16x8 (&fa)[NR], bf16x8 (&fb)[NC]) {
#pragma unroll
    for (int i = 0; i < NR; ++i)
#pragma unroll
      for (int j = 0; j < NC; ++j) {
        if (C::SWAP) acc[i][j] = MFMA16(fb[j], fa[i], acc[i][j]);
        else acc[i][j] = MFMA16(fa[i], fb[j], acc[i][j]);
      }
    if (STATS >= 1) st[4] = MFMA16(ones, fb[0], st[4]);
    if (STATS >= 2) {
#pragma unroll
      for (int i = 0; i < 2; ++i) { st[i] = MFMA16(fa[i], ones, st[i]); st[2 + i] = MFMA16(fa[i], fa[i], st[2 + i]); }
    }
  };
  constexpr int NRD = NR + (C::BMODE == 0 ? 2 : 1) * NC, NMM = NR * NC + (STATS >= 1 ? 1 : 0) + (STATS >= 2 ? 4 : 0);
  constexpr int RPM = (NRD + NMM - 1) / NMM;
  auto ilv = [&]() {
#pragma unroll
    for (int k = 0; k < (NRD + RPM - 1) / RPM; ++k) {
      __builtin_amdgcn_sched_group_barrier(0x008, 1, 0);
      __builtin_amdgcn_sched_group_barrier(0x100, RPM, 0);
    }
    __builtin_amdgcn_sched_group_barrier(0x008, 32, 0);
  };
  PROF(6);
  rawbar();
  PROF(7);
  readf(fa0, fb0, 0, 0);
  SBAR();
  unsigned so = 0;
#pragma unroll 1
  for (int s = 0; s < C::NST - 1; ++s) {
    readf(fa1, fb1, so, 1);
    mma(fa0, fb0);
    ilv();
    SBAR();
    so = so + C::SLOT == C::NSLOT * C::SLOT ? 0u : so + C::SLOT;
    rawbar();
    SBAR();
    readf(fa0, fb0, so, 0);
    mma(fa1, fb1);
    ilv();
    SBAR();
  }
  readf(fa1, fb1, so, 1);
  mma(fa0, fb0);
  ilv();
  SBAR();
  mma(fa1, fb1);
}

template <class C, int NR>
DI void gemm_core_ks(char* smem, unsigned sbase, int lane, int kg, int rt0, const unsigned (&bb)[4], int xme, int xpartner, f32x4 (&out)[5][2] PROF_ARGS) {
  static_assert(C::NST % 2 == 0 && C::AMODE == 0 && C::BMODE == 0, "");
  const unsigned a0 = sbase + (kg * C::NRT + rt0) * 1024 + lane * 16;
  const unsigned bo = kg * 32 * C::BRS;
  f32x4 acc[NR][4];
#pragma unroll
  for (int i = 0; i < NR; ++i)
#pragma unroll
    for (int j = 0; j < 4; ++j) acc[i][j] = f32x4{0, 0, 0, 0};
  bf16x8 fa0[NR], fb0[4], fa1[NR], fb1[4];
  auto readf = [&](bf16x8 (&fa)[NR], bf16x8 (&fb)[4], unsigned so) {
    fa[0] = ldsread16(a0 + so);
#pragma unroll
    for (int j = 0; j < 4; ++j) fb[j] = cat8(trread(bb[j] + so + bo), trread(bb[j] + so + bo + 8 * C::BRS));
#pragma unroll
    for (int i = 1; i < NR; ++i) fa[i] = ldsread16(a0 + so + i * 1024);
  };
  auto mma = [&](bf16x8 (&fa)[NR], bf16x8 (&fb)[4]) {
#pragma unroll
    for (int i = 0; i < NR; ++i)
#pragma unroll
      for (int j = 0; j < 4; ++j) {
        if (C::SWAP) acc[i][j] = MFMA16(fb[j], fa[i], acc[i][j]);
        else acc[i][j] = MFMA16(fa[i], fb[j], acc[i][j]);
      }
  };
  auto ilv = [&]() {
#pragma unroll
    for (int k = 0; k < NR + 8; ++k) {
      __builtin_amdgcn_sched_group_barrier(0x008, 1, 0);
      __builtin_amdgcn_sched_group_barrier(0x100, 1, 0);
    }
    __builtin_amdgcn_sched_group_barrier(0x008, 32, 0);
  };
  auto nxt = [&](unsigned so) { return so + C::SLOT == C::NSLOT * C::SLOT ? 0u : so + C::SLOT; };
  PROF(6);
  rawbar();
  PROF(7);
  readf(fa0, fb0, 0);
  SBAR();
  unsigned so = 0;
#pragma unroll 1
  for (int s = 0; s < C::NST - 2; s += 2) {
    so = nxt(so);
    rawbar(); SBAR();
    readf(fa1, fb1, so); mma(fa0, fb0); ilv(); SBAR();
    so = nxt(so);
    rawbar(); SBAR();
    readf(fa0, fb0, so); mma(fa1, fb1); ilv(); SBAR();
  }
  so = nxt(so);
  rawbar(); SBAR();
  readf(fa1, fb1, so); mma(fa0, fb0); ilv(); SBAR();
  mma(fa1, fb1);
  bar();
  f32x4* mine = (f32x4*)(smem + xme * 10240) + lane;
#pragma unroll
  for (int i = 0; i < NR; ++i)
#pragma unroll
    for (int j = 0; j < 2; ++j) mine[(i * 2 + j) * 64] = kg ? acc[i][j] : acc[i][2 + j];
  bar();
  const f32x4* theirs = (const f32x4*)(smem + xpartner * 10240) + lane;
#pragma unroll
  for (int i = 0; i < NR; ++i)
#pragma unroll
    for (int j = 0; j < 2; ++j) out[i][j] = (kg ? acc[i][2 + j] : acc[i][j]) + theirs[(i * 2 + j) * 64];
}

#define K1_TOUCH 0
#define K1_DF 0
#define K1_PART 6
struct C1 { static constexpr int BMODE = 0, SEG = 9, NST = 9, NSLOT = 5, PART = K1_PART, DF = K1_DF, ABYTES = 16384, BRS = 224, SLOT = ABYTES + 64 * BRS, AMODE = 1, NRT = 8;
            static constexpr int ORD[8] = {0, 4, 1, 5, 2, 6, 3, 7};
            static constexpr int LA = 4, LASTA = 64, LB = 4, LASTB = 32, SA = 128, SB = 64 * 3 * D_ * 2;
            static constexpr bool SWAP = false, ANT = false, BNT = false; };
constexpr int K1_T2RS = 264;
__global__ __launch_bounds__(768) void k1_qkv(const unsigned short* __restrict__ x, const unsigned short* __restrict__ wqkv,
                                              char* __restrict__ ws) {
  __shared__ __attribute__((aligned(16))) char smem[C1::NSLOT * C1::SLOT + 1536 + 256];
  const int b = blockIdx.x, xg = b & 7, idx = b >> 3;
  const int rb = (xg >> 1) * 4 + (idx >> 3);
  const int ct = (xg & 1) * 8 + (idx & 7);
  const int tid = threadIdx.x, lane = tid & 63;
  const int w = __builtin_amdgcn_readfirstlane(tid >> 6);
  const int r = lane & 15, g = lane >> 4;
  const int win0 = ct * 108 - (ct & 1) * 4;
  const int coff = (ct & 1) * 4;
  const unsigned sbase = lds_addr(smem);
  PROF_DECL PROF(0);

  if (w >= 8) {
    const int j = w - 8;
    unsigned oa[4], ob[4];
#pragma unroll
    for (int i = 0; i < 4; ++i) {
      const int row = (i * 4 + j) * 8 + (lane >> 3);
      oa[i] = row * (D_ * 2) + (((lane & 7) ^ (row & 7)) * 16);
    }
#pragma unroll
    for (int i = 0; i < 4; ++i) {
      const int c = i < 3 ? (i * 4 + j) * 64 + lane : 768 + j * 32 + (lane & 31);
      const int rho = c / 14, cc = c - rho * 14;
      ob[i] = swap23(rho) * (3 * D_ * 2) + cc * 16;
    }
    PROF(1);
    gemm_loader<C1>(sbase, j, lane, (const char*)(x + (size_t)rb * 128 * D_), (const char*)(wqkv + win0), oa, ob, K1_ROT ? ((idx & 7) + 2 * (idx >> 3)) % 9 : 0);
    PROF(2); PROF_FLUSH(1);
    return;
  }
  {
    const int gt = b * 512 + tid;
    if (gt < 16384) {
      auto rs = __builtin_amdgcn_make_buffer_rsrc(ws + WS_STAT, 0, 262144, 0x00020000);
      __builtin_amdgcn_raw_buffer_store_b128(u32x4{0xffffffffu, 0xffffffffu, 0xffffffffu, 0xffffffffu}, rs, gt * 16, 0, 16);
    }
  }
  float* csum = (float*)(smem + C1::NSLOT * C1::SLOT);
  float* rstat = (float*)(smem + C1::NSLOT * C1::SLOT + 512);
  const int wm = w & 3, wn = w >> 2;
  const int q4 = (lane & 15) >> 2, p4 = lane & 3;
  const unsigned bbase = sbase + C1::ABYTES + (16 * (g >> 1) + 4 * (g & 1) + q4) * C1::BRS + p4 * 8;
  f32x4 acc[2][4], st[5];
#pragma unroll
  for (int i = 0; i < 2; ++i)
#pragma unroll
    for (int j = 0; j < 4; ++j) acc[i][j] = f32x4{0, 0, 0, 0};
#pragma unroll
  for (int i = 0; i < 5; ++i) st[i] = f32x4{0, 0, 0, 0};
  int ctile[4];
  if (wn == 0) {
    unsigned bb[4];
#pragma unroll
    for (int j = 0; j < 4; ++j) { ctile[j] = (wm + j) & 3; bb[j] = bbase + ctile[j] * 32; }
    gemm_core<C1, 2, 4, 1>(sbase, lane, wm * 2, bb, acc, st PROF_PASS);
  } else {
    unsigned bb[3];
#pragma unroll
    for (int j = 0; j < 3; ++j) { int c = (wm == 3 ? 0 : wm) + j; c = c >= 3 ? c - 3 : c; ctile[j] = 4 + c; bb[j] = bbase + ctile[j] * 32; }
    ctile[3] = 7;
    gemm_core<C1, 2, 3, 2>(sbase, lane, wm * 2, bb, acc, st PROF_PASS);
    const int e = r - 4 * g;
#pragma unroll
    for (int i = 0; i < 2; ++i) {
      float q0 = st[2 + i][0], q1 = st[2 + i][1], q2 = st[2 + i][2], q3 = st[2 + i][3];
      float s0 = st[i][0], s1 = st[i][1], s2 = st[i][2], s3 = st[i][3];
      asm volatile("" : "+v"(q0), "+v"(q1), "+v"(q2), "+v"(q3), "+v"(s0), "+v"(s1), "+v"(s2), "+v"(s3));
      const float sq = (e & 2) ? ((e & 1) ? q3 : q2) : ((e & 1) ? q1 : q0);
      const float sm = (e & 2) ? ((e & 1) ? s3 : s2) : ((e & 1) ? s1 : s0);
      const float m = sm * (1.0f / D_);
      const float var = fmaxf(sq * (1.0f / D_) - m * m, 0.f);
      if (e >= 0 && e < 4) *(f32x2*)(rstat + ((wm * 2 + i) * 16 + r) * 2) = f32x2{m, __builtin_amdgcn_rsqf(var + 1e-5f)};
    }
  }
  PROF(1);
  if (g == 0) csum[ctile[0] * 16 + r] = st[4][0];
  bar();
#pragma unroll
  for (int i = 0; i < 2; ++i) {
    const f32x4 s01 = *(const f32x4*)(rstat + ((wm * 2 + i) * 16 + 4 * g) * 2), s23 = *(const f32x4*)(rstat + ((wm * 2 + i) * 16 + 4 * g + 2) * 2);
#pragma unroll
    for (int j = 0; j < 4; ++j) {
      if (j < 3 || wn == 0) {
        const int col = ctile[j] * 16 + r;
        const float c_ = csum[col];
        const float sc = win0 + col < D_ ? 0.34004502f   : 1.0f;
        float v0 = (acc[i][j][0] - s01[0] * c_) * (s01[1] * sc), v1 = (acc[i][j][1] - s01[2] * c_) * (s01[3] * sc);
        float v2 = (acc[i][j][2] - s23[0] * c_) * (s23[1] * sc), v3 = (acc[i][j][3] - s23[2] * c_) * (s23[3] * sc);
        *(u32x2*)(smem + col * K1_T2RS + ((wm * 2 + i) * 16 + 4 * g) * 2) = u32x2{pack2(v0, v1), pack2(v2, v3)};
      }
    }
  }
  bar();
  PROF(2);
  if (w < 6) {
    const int slice = ct * 6 + w;
    const int type = slice >> 5, head = slice & 31;
    const int c0 = coff + 18 * w;
    if (type < 2) {
      const int hh = g >> 1;
      char* dstA = type == 0 ? ws + WS_QA + ((size_t)head * 64 + rb * 4) * 1024 : ws + WS_KV + head * KV_HEAD + rb * 9984;
      char* dstB = type == 0 ? ws + WS_QB + ((size_t)head * 64 + rb * 4) * 256 : ws + WS_KV + head * KV_HEAD + rb * 9984 + 4096;
      u32x4 u[4]; u32x2 ub[2];
      const unsigned ad = sbase + (c0 + 8 * hh + q4) * K1_T2RS + ((g & 1) * 16 + 4 * p4) * 2;
      const unsigned adb = sbase + (c0 + 16 + q4) * K1_T2RS + (g * 16 + 4 * p4) * 2;
#pragma unroll
      for (int t = 0; t < 4; ++t) u[t] = __builtin_bit_cast(u32x4, cat8(trread(ad + t * 64), trread(ad + t * 64 + 4 * K1_T2RS)));
#pragma unroll
      for (int t = 0; t < 2; ++t) { ub[t] = __builtin_bit_cast(u32x2, trread(adb + t * 128)); ub[t][1] = type == 1 ? 0x00003f80u : 0u; }
#pragma unroll
      for (int t = 0; t < 4; ++t) *(u32x4*)(dstA + t * 1024 + lane * 16) = u[t];
#pragma unroll
      for (int t = 0; t < 2; ++t) *(u32x2*)(dstB + t * 512 + lane * 8) = ub[t];
      if (type == 1) {
        float nmax = 0.f;
#pragma unroll
        for (int t = 0; t < 4; ++t) nmax = fmaxf(nmax, dot2(u[t][0], u[t][0], dot2(u[t][1], u[t][1], dot2(u[t][2], u[t][2], dot2(u[t][3], u[t][3], 0.f)))));
        float nmb = fmaxf(dot2(ub[0][0], ub[0][0], 0.f), dot2(ub[1][0], ub[1][0], 0.f));
#pragma unroll
        for (int o = 1; o < 32; o <<= 1) { nmax = fmaxf(nmax, __shfl_xor(nmax, o)); nmb = fmaxf(nmb, __shfl_xor(nmb, o)); }
        nmb = fmaxf(nmb, __shfl_xor(nmb, 32));
        float* km = (float*)(ws + WS_KMAX) + head * 48;
        if (lane == 0) { km[rb] = nmax; km[32 + rb] = nmb; }
        if (lane == 32) km[16 + rb] = nmax;
      }
    } else {
      char* dst = ws + WS_KV + head * KV_HEAD + rb * 9984 + 5120;
      u32x4 u[5];
#pragma unroll
      for (int it = 0; it < 5; ++it) {
        int pi = it * 64 + lane; pi = pi < 304 ? pi : 0;
        const int s = pi / 38, rem = pi - s * 38, hh = rem >= 19 ? 1 : 0, d = rem - hh * 19;
        const char* src = smem + (c0 + (d == 18 ? 0 : d)) * K1_T2RS + (s * 16 + 4 * hh) * 2;
        const u32x2 lo = *(const u32x2*)src, hi = *(const u32x2*)(src + 16);
        u[it] = d == 18 ? u32x4{0x3f803f80u, 0x3f803f80u, 0x3f803f80u, 0x3f803f80u} : u32x4{lo[0], lo[1], hi[0], hi[1]};
      }
#pragma unroll
      for (int it = 0; it < 5; ++it)
        if (it < 4 || lane < 48) *(u32x4*)(dst + (it * 64 + lane) * 16) = u[it];
    }
  }
  PROF(3); PROF_FLUSH(1);
}

#define W1_NT 0
#define W2_K2 4
DI unsigned exp2pk(float a, float b) { return pack2(__builtin_amdgcn_exp2f(a), __builtin_amdgcn_exp2f(b)); }

DI void k2_exact(const char* kvg, int lane, bf16x8 qa, bf16x4 qb0, f32x16& o) {
  const int r = lane & 31, hh = lane >> 5;
  const char* ka_p = kvg + lane * 16;
  const char* kb_p = kvg + 4096 + r * 8;
  const char* v_p = kvg + 5120 + (hh * 19 + (r < 19 ? r : 0)) * 16;
  f32x16 zero16;
#pragma unroll
  for (int i = 0; i < 16; ++i) zero16[i] = 0.f;
  float mx = -3.0e38f;
#pragma unroll 1
  for (int t = 0; t < 64; ++t) {
    const int go = (t >> 2) * 9984;
    f32x16 s = MFMA32(*(const bf16x8*)(ka_p + go + (t & 3) * 1024), qa, zero16);
    s = MFMA32K8(*(const bf16x4*)(kb_p + go + (t & 3) * 256), qb0, s);
#pragma unroll
    for (int e = 0; e < 16; ++e) mx = fmaxf(mx, s[e]);
  }
  mx = fmaxf(mx, __shfl_xor(mx, 32));
  o = zero16;
#pragma unroll 1
  for (int t = 0; t < 64; ++t) {
    const int go = (t >> 2) * 9984;
    bf16x8 v0 = *(const bf16x8*)(v_p + go + (2 * (t & 3)) * 608);
    bf16x8 v1 = *(const bf16x8*)(v_p + go + (2 * (t & 3) + 1) * 608);
    f32x16 s = MFMA32(*(const bf16x8*)(ka_p + go + (t & 3) * 1024), qa, zero16);
    s = MFMA32K8(*(const bf16x4*)(kb_p + go + (t & 3) * 256), qb0, s);
    u32x4 p0, p1;
#pragma unroll
    for (int e = 0; e < 4; ++e) {
      p0[e] = exp2pk(s[2 * e] - mx, s[2 * e + 1] - mx);
      p1[e] = exp2pk(s[8 + 2 * e] - mx, s[8 + 2 * e + 1] - mx);
    }
    o = MFMA32(v0, __builtin_bit_cast(bf16x8, p0), o);
    o = MFMA32(v1, __builtin_bit_cast(bf16x8, p1), o);
  }
}

__global__ __launch_bounds__(768) void k2_attn(char* __restrict__ ws, const unsigned short* __restrict__ wo, const unsigned short* __restrict__ w1,
                                               const unsigned short* __restrict__ w2) {
  __shared__ __attribute__((aligned(16))) char smem[163840];
  const int b = blockIdx.x, xg = b & 7, idx = b >> 3;
  const int head = xg * 4 + (idx >> 3), qblk = idx & 7;
  const int tid = threadIdx.x, lane = tid & 63;
  const int w = __builtin_amdgcn_readfirstlane(tid >> 6);
  const int r = lane & 31, hh = lane >> 5;
  const unsigned sbase = lds_addr(smem);
  const char* kvg = ws + WS_KV + head * KV_HEAD;
  volatile LDSP unsigned* xflag = (volatile LDSP unsigned*)(uintptr_t)(sbase + 163328);
  PROF_DECL PROF(0);

  if (w >= 8) {
    const int j = w - 8;
    const unsigned vo = j * 1024 + lane * 16;
    const char* src = kvg;
    unsigned dst = sbase + j * 1024;
#pragma unroll 1
    for (int i = 0; i < 5; ++i) { glds16(src, vo, dst); src += 4096; dst += 4096; }
    if (j == 0 && lane < 8) xflag[lane] = 0u;
    wait_vm<0>();
    bar();
#pragma unroll 1
    for (int i = 5; i < 15; ++i) { glds16(src, vo, dst); src += 4096; dst += 4096; }
    wait_vm<5>();
    __builtin_amdgcn_s_barrier();
#pragma unroll 1
    for (int i = 15; i < 39; ++i) { glds16(src, vo, dst); src += 4096; dst += 4096; }
    wait_vm<0>();
    __builtin_amdgcn_s_barrier();
    {
      const int f = j * 256 + b;
      if (f < 648) {
        const int c16 = f / 18, k32 = f - c16 * 18;
        const unsigned short* p = wo + (size_t)(k32 * 32 + (lane >> 4) * 8) * D_ + c16 * 16 + (lane & 15);
        u32x4 v;
#pragma unroll
        for (int e = 0; e < 4; ++e) v[e] = (unsigned)p[(2 * e) * D_] | ((unsigned)p[(2 * e + 1) * D_] << 16);
        *(u32x4*)(ws + WS_WOT + (size_t)f * 1024 + lane * 16) = v;
      }
    }
    const unsigned scr = sbase + 163584;
    {
      const char* base = (const char*)w2 + (size_t)((xg >> 1) * 1152 + idx * 36) * (S_ * 2) + (xg & 1) * 2048;
#pragma unroll 1
      for (int q = j; q < W2_K2; q += 4) touch(base, (q * 4 + (lane >> 4)) * (S_ * 2) + (lane & 15) * 128, scr);
      const char* base1 = (const char*)w1 + (size_t)(idx * 64) * (F_ * 2) + ((xg * 27) / 6) * 256;
#pragma unroll 1
      for (int q = j; q < 10; q += 4) {
        const int li = q * 64 + lane, rr = li / 10, c = li - rr * 10;
        if (W1_NT) touch_nt(base1, rr * (F_ * 2) + c * 128, scr); else touch(base1, rr * (F_ * 2) + c * 128, scr);
      }
    }
    wait_vm<0>();
    return;
  }
  const int qp = w & 3, kh = w >> 2;
  const int qt = qblk * 8 + qp * 2;
  u32x4 qa_u[2]; u32x2 qb_u[2]; unsigned km_u;
  gload16(qa_u[0], ws + WS_QA + (((size_t)head * 64 + qt) * 64 + lane) * 16);
  gload16(qa_u[1], ws + WS_QA + (((size_t)head * 64 + qt + 1) * 64 + lane) * 16);
  gload8(qb_u[0], ws + WS_QB + (((size_t)head * 64 + qt) * 32 + r) * 8);
  gload8(qb_u[1], ws + WS_QB + (((size_t)head * 64 + qt + 1) * 32 + r) * 8);
  gload4(km_u, ws + WS_KMAX + (head * 48 + (lane < 48 ? lane : 0)) * 4);
  float km = __uint_as_float(km_u);
#pragma unroll
  for (int o = 1; o < 16; o <<= 1) km = fmaxf(km, __shfl_xor(km, o));
  const float kn2 = __builtin_bit_cast(float, __builtin_amdgcn_readlane(__builtin_bit_cast(int, km), 0)) +
                    __builtin_bit_cast(float, __builtin_amdgcn_readlane(__builtin_bit_cast(int, km), 16)) +
                    __builtin_bit_cast(float, __builtin_amdgcn_readlane(__builtin_bit_cast(int, km), 32));
  bf16x8 qa[2]; bf16x4 qb[2]; unsigned qlo[2];
#pragma unroll
  for (int a = 0; a < 2; ++a) {
    qlo[a] = hh == 0 ? qb_u[a][0] : 0u;
    float qn2 = dot2(qa_u[a][0], qa_u[a][0], dot2(qa_u[a][1], qa_u[a][1], dot2(qa_u[a][2], qa_u[a][2], dot2(qa_u[a][3], qa_u[a][3], dot2(qlo[a], qlo[a], 0.f)))));
    qn2 += __shfl_xor(qn2, 32);
    const float mb = __builtin_amdgcn_sqrtf(qn2 * kn2);
    u32x2 t;
    t[0] = qlo[a];
    t[1] = hh == 0 ? pack2(-mb, 0.f) : 0u;
    qa[a] = __builtin_bit_cast(bf16x8, qa_u[a]);
    qb[a] = __builtin_bit_cast(bf16x4, t);
  }
  const int dsel = r < 19 ? r : ((r >= 20 && r < 28) ? 0 : 4);
  const unsigned ka_p = sbase + lane * 16;
  const unsigned kb_p = sbase + 4096 + r * 8;
  const unsigned v_p = sbase + 5120 + (hh * 19 + dsel) * 16;
  f32x16 oa, ob;
#pragma unroll
  for (int i = 0; i < 16; ++i) { oa[i] = 0.f; ob[i] = 0.f; }
  const f32x16 zero16 = oa;
  PROF(1);
  rawbar();
  PROF(2);
  f32x16 sa0, sb0, sa1, sb1;
  bf16x8 kaN, v1p; bf16x4 kbN; u32x4 p1p = {0, 0, 0, 0};
  unsigned go = kh * 9984u;
  {
    bf16x8 ka0 = ldsread16(ka_p + go); bf16x4 kb0 = ldsread8(kb_p + go);
    kaN = ldsread16(ka_p + go + 1024); kbN = ldsread8(kb_p + go + 256);
    v1p = ldsread16(v_p);
    sa0 = MFMA32(ka0, qa[0], zero16); sb0 = MFMA32(ka0, qa[1], zero16);
    sa0 = MFMA32K8(kb0, qb[0], sa0); sb0 = MFMA32K8(kb0, qb[1], sb0);
  }
#define K2_UNIT(oP, v1P, oC, sC, sN, Q)                                                                \
    SBAR();                                                                                            \
    oP = MFMA32(v1P, __builtin_bit_cast(bf16x8, p1p), oP);                                             \
    SBAR();                                                                                            \
    p0[0] = exp2pk(sC[0], sC[1]); p0[1] = exp2pk(sC[2], sC[3]);                                        \
    SBAR();                                                                                            \
    sN = MFMA32(kaN, qa[Q], zero16);                                                                   \
    SBAR();                                                                                            \
    p0[2] = exp2pk(sC[4], sC[5]); p0[3] = exp2pk(sC[6], sC[7]);                                        \
    SBAR();                                                                                            \
    sN = MFMA32K8(kbN, qb[Q], sN);                                                                     \
    SBAR();                                                                                            \
    p1p[0] = exp2pk(sC[8], sC[9]); p1p[1] = exp2pk(sC[10], sC[11]);                                    \
    SBAR();                                                                                            \
    oC = MFMA32(v0, __builtin_bit_cast(bf16x8, p0), oC);                                               \
    SBAR();                                                                                            \
    p1p[2] = exp2pk(sC[12], sC[13]); p1p[3] = exp2pk(sC[14], sC[15]);                                  \
    SBAR();
#define K2_TILE(saC, sbC, saN, sbN, KOFF_A, KOFF_B, VOFF)                                              \
  {                                                                                                    \
    bf16x8 v0 = ldsread16(v_p + go + (VOFF)), v1 = ldsread16(v_p + go + (VOFF) + 608);                 \
    bf16x8 ka2 = ldsread16(ka_p + go + (KOFF_A)); bf16x4 kb2 = ldsread8(kb_p + go + (KOFF_B));         \
    u32x4 p0;                                                                                          \
    K2_UNIT(ob, v1p, oa, saC, saN, 0)                                                                  \
    K2_UNIT(oa, v1, ob, sbC, sbN, 1)                                                                   \
    v1p = v1; kaN = ka2; kbN = kb2;                                                                    \
  }
#define K2_PRIO(i) if (K2_PHI > 0 && kh) __builtin_amdgcn_s_setprio((i) < K2_PHI ? 1 : 0);
#define K2_QUAD                                                                                        \
  K2_PRIO(0) K2_TILE(sa0, sb0, sa1, sb1, 19968, 19968, 2432)                                           \
  K2_PRIO(1) K2_TILE(sa1, sb1, sa0, sb0, 19968 + 1024, 19968 + 256, 3648)                              \
  K2_PRIO(2) K2_TILE(sa0, sb0, sa1, sb1, 19968 + 2048, 19968 + 512, 19968)                             \
  K2_PRIO(3) K2_TILE(sa1, sb1, sa0, sb0, 19968 + 3072, 19968 + 768, 19968 + 1216)
  K2_TILE(sa0, sb0, sa1, sb1, 2048, 512, 0)
  K2_TILE(sa1, sb1, sa0, sb0, 3072, 768, 1216)
  rawbar();
  K2_QUAD
  bar(); PROF(3);
#pragma unroll 1
  for (int it = 0; it < 6; ++it) { go += 19968u; K2_QUAD }
  go += 19968u;
  K2_TILE(sa0, sb0, sa1, sb1, 0, 0, 2432)
  K2_TILE(sa1, sb1, sa0, sb0, 1024, 256, 3648)
#undef K2_QUAD
#undef K2_TILE
#undef K2_UNIT
  ob = MFMA32(v1p, __builtin_bit_cast(bf16x8, p1p), ob);
  PROF(4);
  f32x16 of, og;
  if (kh) { of = ob; og = oa; } else { of = oa; og = ob; }
  {
    f32x4* xch = (f32x4*)(smem + w * 2560) + lane;
    xch[0] = f32x4{og[0], og[1], og[2], og[3]}; xch[64] = f32x4{og[4], og[5], og[6], og[7]};
    if (hh == 0) xch[128] = f32x4{og[8], og[9], og[10], og[11]};
    asm volatile("s_waitcnt lgkmcnt(0)" ::: "memory");
    xflag[w] = 1u;
    while (__builtin_amdgcn_readfirstlane(xflag[w ^ 4]) == 0u) __builtin_amdgcn_s_sleep(1);
    asm volatile("" ::: "memory");
    const f32x4* pch = (const f32x4*)(smem + (w ^ 4) * 2560) + lane;
    const f32x4 x0 = pch[0], x1 = pch[64], x2 = pch[hh == 0 ? 128 : 0];
#pragma unroll
    for (int i = 0; i < 4; ++i) { of[i] += x0[i]; of[4 + i] += x1[i]; of[8 + i] += x2[i]; }
  }
  float l = __shfl(of[10], r);
  if (__builtin_expect(__any(!(l >= 5.421e-20f)), 0)) {
    u32x2 qz; qz[0] = kh ? qlo[1] : qlo[0]; qz[1] = 0u;
    k2_exact(kvg, lane, kh ? qa[1] : qa[0], __builtin_bit_cast(bf16x4, qz), of);
    l = __shfl(of[10], r);
  }
  const float rl = 1.0f / l;
  {
    const int q = (qt + kh) * 32 + r;
    char* obase = ws + WS_O + (size_t)(q >> 4) * 18432 + (q & 15) * 16;
    auto st2 = [&](int d, float a0, float a1) {
      const int col = head * DH_ + d;
      *(unsigned*)(obase + (col >> 3) * 256 + (col & 7) * 2) = pack2(a0 * rl, a1 * rl);
    };
    st2(4 * hh, of[0], of[1]); st2(4 * hh + 2, of[2], of[3]);
    st2(8 + 4 * hh, of[4], of[5]); st2(8 + 4 * hh + 2, of[6], of[7]);
    if (hh == 0) st2(16, of[8], of[9]);
  }
  PROF(5); PROF_FLUSH(2);
}

#define K3_DF 0
#define K3_PART 5
struct C3 { static constexpr int BMODE = 1, SEG = 9, NST = 9, NSLOT = 8, PART = K3_PART, DF = K3_DF, ABYTES = 8192, BRS = 0, SLOT = ABYTES + 10240, AMODE = 0, NRT = 4;
            static constexpr int ORD[5] = {0, 2, 1, 3, 4};
            static constexpr int LA = 2, LASTA = 64, LB = 3, LASTB = 32, SA = 2048, SB = 2048;
            static constexpr bool SWAP = true, ANT = false, BNT = false; };
template <int NC>
DI void k3_epilogue(char* smem, unsigned sbase, int lane, int w, int wm, int wn, int rb, int ct, f32x4 (&acc)[1][3],
                    const u32x2 (&xr)[3], char* ws PROF_ARGS) {
  const int r = lane & 15, g = lane >> 4, q4 = (lane & 15) >> 2, p4 = lane & 3;
  const int token = rb * 64 + wm * 16 + r;
  const int c0 = wn * 3, coff = (ct & 1) * 8, win0 = ct * 72 - coff;
  float hv[NC][4];
  u32x2 hp[NC];
  float sum = 0.f;
#pragma unroll
  for (int c = 0; c < NC; ++c) {
    const bool valid = (unsigned)((c0 + c) * 16 + 4 * g - coff) < 72u;
    hp[c][0] = pack2(bflo(xr[c][0]) + acc[0][c][0], bfhi(xr[c][0]) + acc[0][c][1]);
    hp[c][1] = pack2(bflo(xr[c][1]) + acc[0][c][2], bfhi(xr[c][1]) + acc[0][c][3]);
    hv[c][0] = bflo(hp[c][0]); hv[c][1] = bfhi(hp[c][0]); hv[c][2] = bflo(hp[c][1]); hv[c][3] = bfhi(hp[c][1]);
    if (valid) sum += (hv[c][0] + hv[c][1]) + (hv[c][2] + hv[c][3]);
  }
  sum += __shfl_xor(sum, 16); sum += __shfl_xor(sum, 32);
  const float lm = sum * (NC == 3 ? (coff ? 1.0f / 40 : 1.0f / 48) : (coff ? 1.0f / 32 : 1.0f / 24));
  float m2 = 0.f;
#pragma unroll
  for (int c = 0; c < NC; ++c) {
    if ((unsigned)((c0 + c) * 16 + 4 * g - coff) < 72u) {
#pragma unroll
      for (int i = 0; i < 4; ++i) { float d = hv[c][i] - lm; m2 += d * d; }
    }
  }
  m2 += __shfl_xor(m2, 16); m2 += __shfl_xor(m2, 32);
  unsigned long long* st = (unsigned long long*)(ws + WS_STAT) + (size_t)rb * 1024;
  {
    unsigned long long v = ((unsigned long long)__float_as_uint(m2) << 32) | __float_as_uint(lm);
    if (v == STAT_SENT) v = 0x7fc000007fc00000ull;
    if (g == 0) __hip_atomic_store(st + (ct * 2 + wn) * 64 + wm * 16 + r, v, __ATOMIC_RELAXED, __HIP_MEMORY_SCOPE_AGENT);
  }
  {
    unsigned short* hrow = (unsigned short*)(ws + WS_H) + (size_t)token * D_ + win0 + 4 * g;
#pragma unroll
    for (int c = 0; c < NC; ++c)
      if ((unsigned)((c0 + c) * 16 + 4 * g - coff) < 72u) *(u32x2*)(hrow + (c0 + c) * 16) = hp[c];
  }
  PROF(3);
  unsigned long long sv[4];
  {
    const unsigned long long* p0 = st + (4 * g) * 64 + wm * 16 + r;
    for (unsigned spin = 0; spin < (1u << 24); ++spin) {
      bool ok = true;
#pragma unroll
      for (int k = 0; k < 4; ++k) { sv[k] = __hip_atomic_load(p0 + k * 64, __ATOMIC_RELAXED, __HIP_MEMORY_SCOPE_AGENT); ok &= sv[k] != STAT_SENT; }
      if (__all(ok)) break;
      __builtin_amdgcn_s_sleep(1);
    }
  }
  PROF(4);
  constexpr float NK[4] = {48.f, 24.f, 40.f, 32.f};
  float msum = 0.f, mk[4];
#pragma unroll
  for (int k = 0; k < 4; ++k) { mk[k] = __uint_as_float((unsigned)sv[k]); msum += NK[k] * mk[k]; }
  msum += __shfl_xor(msum, 16); msum += __shfl_xor(msum, 32);
  const float mean = msum * (1.0f / D_);
  float dv = 0.f;
#pragma unroll
  for (int k = 0; k < 4; ++k) { float d = mk[k] - mean; dv += __uint_as_float((unsigned)(sv[k] >> 32)) + NK[k] * d * d; }
  dv += __shfl_xor(dv, 16); dv += __shfl_xor(dv, 32);
  const float var = dv * (1.0f / D_);
  const float r1 = __builtin_amdgcn_rsqf(var + 1e-5f);
  const float r2 = __builtin_amdgcn_rsqf(var * r1 * r1 + 1e-5f);
  const float rr = r1 * r2;
  constexpr int TRS = 168;
  smem += C3::SLOT; sbase += C3::SLOT;
#pragma unroll
  for (int c = 0; c < NC; ++c) {
    u32x2 pk = {pack2((hv[c][0] - mean) * rr, (hv[c][1] - mean) * rr), pack2((hv[c][2] - mean) * rr, (hv[c][3] - mean) * rr)};
    *(u32x2*)(smem + (wm * 16 + r) * TRS + ((c0 + c) * 16 + 4 * g) * 2) = pk;
  }
  bar();
  for (int it = w; it < 10; it += 8) {
    const int kk = it / 5, c = it - kk * 5;
    unsigned ad = sbase + (kk * 32 + 8 * g + q4) * TRS + (c * 16 + 4 * p4) * 2;
    bf16x8 v = cat8(trread(ad), trread(ad + 4 * TRS));
    const int col = c * 16 + r;
    if ((unsigned)(col - coff) < 72u) {
      const int d = win0 + col;
      const int mt = d / 96, dd = d - mt * 96;
      const int k32 = rb * 2 + kk;
      char* dst = ws + WS_HNT + ((((size_t)mt * 64 + k32) * 6 + (dd >> 4)) * 64 + g * 16 + (dd & 15)) * 16;
      *(u32x4*)dst = __builtin_bit_cast(u32x4, v);
    }
  }
}

__global__ __launch_bounds__(768) void k3_oproj(const unsigned short* __restrict__ x, char* __restrict__ ws) {
  __shared__ __attribute__((aligned(16))) char smem[C3::NSLOT * C3::SLOT + 256];
  const int b = blockIdx.x, xg = b & 7, idx = b >> 3;
  const int rb = xg * 4 + (idx >> 3), ct = idx & 7;
  const int tid = threadIdx.x, lane = tid & 63;
  const int w = __builtin_amdgcn_readfirstlane(tid >> 6);
  const int r = lane & 15, g = lane >> 4;
  const unsigned sbase = lds_addr(smem);
  PROF_DECL PROF(0);
  if (w >= 8) {
    const int j = w - 8;
    unsigned oa[2], ob[3];
#pragma unroll
    for (int i = 0; i < 2; ++i) oa[i] = (j * 18 + i) * 1024 + lane * 16;
#pragma unroll
    for (int i = 0; i < 2; ++i) { const int q = i * 4 + j; ob[i] = (q >> 1) * 18432 + (q & 1) * 1024 + lane * 16; }
    ob[2] = 4 * 18432 + j * 512 + (lane & 31) * 16;
    PROF(1);
    gemm_loader<C3>(sbase, j, lane, ws + WS_O + (size_t)rb * (4 * 18432), ws + WS_WOT + (size_t)((ct * 9) >> 1) * 18432, oa, ob, K3_ROT ? (ct + 2 * (idx >> 3)) % 9 : 0);
    PROF(2); PROF_FLUSH(3);
    return;
  }
  const int wm = w & 3, wn = w >> 2;
  u32x2 xr[3];
  {
    const char* xrow = (const char*)(x + (size_t)(rb * 64 + wm * 16 + r) * D_ + ct * 72 - (ct & 1) * 8 + wn * 48 + 4 * g);
    gload8(xr[0], xrow); gload8(xr[1], xrow + 32); gload8(xr[2], xrow + (wn == 0 ? 64 : 0));
  }
  const unsigned bbase = sbase + C3::ABYTES + wn * 6144 + lane * 16;
  f32x4 acc[1][3], st[5];
#pragma unroll
  for (int j = 0; j < 3; ++j) acc[0][j] = f32x4{0, 0, 0, 0};
  if (wn == 0) {
    unsigned bb[3] = {bbase, bbase + 2048, bbase + 4096};
    gemm_core<C3, 1, 3, 0>(sbase, lane, wm, bb, acc, st PROF_PASS);
    PROF(1);
    k3_epilogue<3>(smem, sbase, lane, w, wm, wn, rb, ct, acc, xr, ws PROF_PASS);
  } else {
    unsigned bb[2] = {bbase, bbase + 2048};
    gemm_core<C3, 1, 2, 0>(sbase, lane, wm, bb, acc, st PROF_PASS);
    PROF(1);
    k3_epilogue<2>(smem, sbase, lane, w, wm, wn, rb, ct, acc, xr, ws PROF_PASS);
  }
  PROF(2); PROF_FLUSH(3);
}

#define K4_ANT false
#define K4_BNT false
#define K4_AT0 3
#define K4_AT 0
#define K4_DF 1
#define K4_PART 7
struct C4 { static constexpr int BMODE = 0, SEG = K4_SEG, NST = 32, NSLOT = 5, PART = K4_PART, DF = K4_DF, ABYTES = 12288, BRS = 256, SLOT = ABYTES + 64 * BRS, AMODE = 0, NRT = 6;
            static constexpr int ORD[7] = {0, 3, 1, 4, 2, 5, 6};
            static constexpr int LA = 3, LASTA = 64, LB = 4, LASTB = 64, SA = 12288, SB = 64 * F_ * 2;
            static constexpr bool SWAP = true, ANT = K4_ANT, BNT = K4_BNT; };
DI float gelu_tanh(float x) {
  float u = x * (0.7978845608028654f + 0.0356774081363001f * x * x);
  float e = __builtin_amdgcn_exp2f(-2.8853900817779268f * u);
  return x * __builtin_amdgcn_rcpf(1.0f + e);
}
__global__ __launch_bounds__(768) void k4_gemm1(const unsigned short* __restrict__ w1, const unsigned short* __restrict__ b1,
                                                const unsigned short* __restrict__ w2, char* __restrict__ ws) {
  __shared__ __attribute__((aligned(16))) char smem[C4::NSLOT * C4::SLOT];
  const int b = blockIdx.x, xg = b & 7, idx = b >> 3;
  const int tid = threadIdx.x, lane = tid & 63;
  const int w = __builtin_amdgcn_readfirstlane(tid >> 6);
  const unsigned sbase = lds_addr(smem);
  PROF_DECL PROF(0);
  if (idx >= 27) {
    const int sw = (idx - 27) * 12 + w;
    {
      auto rs = __builtin_amdgcn_make_buffer_rsrc(ws + WS_P5, 0, 256 * P5_SLOT, 0x00020000);
      const u32x4 sent = {0xffffffffu, 0xffffffffu, 0xffffffffu, 0xffffffffu};
#pragma unroll 1
      for (int q = xg * 60 + sw; q < 9216; q += 480) __builtin_amdgcn_raw_buffer_store_b128(sent, rs, q * 1024 + lane * 16, 0, 16);
    }
    constexpr int PER = 9 - W2_K2;
    const char* base = (const char*)w2 + (size_t)((xg >> 1) * 1152) * (S_ * 2) + (xg & 1) * 2048;
#pragma unroll 1
    for (int q = sw; q < 32 * PER; q += 60) {
      const int ch = q / (PER > 0 ? PER : 1);
      touch(base, (ch * 36 + (W2_K2 + q - ch * PER) * 4 + (lane >> 4)) * (S_ * 2) + (lane & 15) * 128, sbase);
    }
    wait_vm<0>();
    return;
  }
  const int t = xg * 27 + idx;
  const int ntile = t / 6, mt = t - ntile * 6;
  const int r = lane & 15, g = lane >> 4;
  if (w >= 8) {
    const int j = w - 8;
    unsigned oa[3], ob[4];
#pragma unroll
    for (int i = 0; i < 3; ++i) oa[i] = (i * 4 + j) * 1024 + lane * 16;
#pragma unroll
    for (int i = 0; i < 4; ++i) {
      const int rho = (i * 4 + j) * 4 + (lane >> 4), cc = (lane & 15) ^ ((rho & 7) << 1);
      ob[i] = swap23(rho) * (F_ * 2) + cc * 16;
    }
    gemm_loader<C4>(sbase, j, lane, ws + WS_HNT + (size_t)mt * (64 * 6144), (const char*)(w1 + ntile * 128), oa, ob, 0);
    return;
  }
  const int wm = w >> 2, wn = w & 3;
  u32x2 bias[2];
  gload8(bias[0], b1 + ntile * 128 + wn * 32 + 4 * g); gload8(bias[1], b1 + ntile * 128 + wn * 32 + 16 + 4 * g);
  const int q4 = (lane & 15) >> 2, p4 = lane & 3;
  const int rho = 16 * (g >> 1) + 4 * (g & 1) + q4;
  unsigned bb[2];
#pragma unroll
  for (int j = 0; j < 2; ++j) bb[j] = sbase + C4::ABYTES + rho * 256 + (((wn * 2 + j) ^ (rho & 7)) * 32) + p4 * 8;
  f32x4 acc[3][2], st[5];
#pragma unroll
  for (int i = 0; i < 3; ++i)
#pragma unroll
    for (int j = 0; j < 2; ++j) acc[i][j] = f32x4{0, 0, 0, 0};
  PROF(1);
  gemm_core<C4, 3, 2, 0>(sbase, lane, wm * 3, bb, acc, st PROF_PASS);
  PROF(2);
#pragma unroll
  for (int i = 0; i < 3; ++i) {
    const int d = mt * 96 + (wm * 3 + i) * 16 + r;
    const int mb = d / 144, dd = d - mb * 144;
    char* dst = ws + WS_U + ((((size_t)mb * 144 + ntile * 4 + wn) * 9 + (dd >> 4)) * 64 + (g >> 1) * 16 + (dd & 15)) * 16 + (g & 1) * 8;
#pragma unroll
    for (int j = 0; j < 2; ++j) {
      const f32x4 v = acc[i][j];
      float y0 = gelu_tanh(v[0] + bflo(bias[j][0])), y1 = gelu_tanh(v[1] + bfhi(bias[j][0]));
      float y2 = gelu_tanh(v[2] + bflo(bias[j][1])), y3 = gelu_tanh(v[3] + bfhi(bias[j][1]));
      *(u32x2*)(dst + j * 512) = u32x2{pack2(y0, y1), pack2(y2, y3)};
    }
  }
  PROF(3); PROF_FLUSH(4);
}

#define K5_CAP (1u << 22)
#define K5_PART 9
struct C5 { static constexpr int BMODE = 0, SEG = K5_SEG, NST = 18, NSLOT = 4, PART = K5_PART, DF = 0, ABYTES = 18432, BRS = 256, SLOT = ABYTES + 64 * BRS, AMODE = 0, NRT = 9;
            static constexpr int ORD[9] = {0, 5, 1, 6, 2, 7, 3, 8, 4};
            static constexpr int LA = 5, LASTA = 32, LB = 4, LASTB = 64, SA = 18432, SB = 64 * S_ * 2;
            static constexpr bool SWAP = false, ANT = false, BNT = false; };
DI unsigned pack2s(float lo, float hi) { const unsigned v = pack2(lo, hi); return v == 0xffffffffu ? 0x7fc07fc0u : v; }
template <int NR>
DI void k5_epilogue(int tile, int kq, int w, int lane, int mblk, int ntile, int rt0, int ct0, f32x4 (&acc)[5][2], const unsigned short* b2,
                    char* ws, unsigned short* out PROF_ARGS) {
  const int r = lane & 15, g = lane >> 4;
  auto rs = __builtin_amdgcn_make_buffer_rsrc(ws + WS_P5, 0, 256 * P5_SLOT, 0x00020000);
  constexpr int HALF = (NR + 1) / 2;
#pragma unroll
  for (int o = 0; o < 4; ++o) {
    if (o != kq) {
      const int j = o >> 1, i0 = (o & 1) ? HALF : 0, cnt = (o & 1) ? NR - HALF : HALF;
      const int base = (tile * 4 + o) * P5_SLOT + (w * 3 + ((kq - o - 1) & 3)) * 1536;
      u32x4 v = {pack2s(acc[i0][j][0], acc[i0][j][1]), pack2s(acc[i0][j][2], acc[i0][j][3]),
                 pack2s(acc[i0 + 1][j][0], acc[i0 + 1][j][1]), pack2s(acc[i0 + 1][j][2], acc[i0 + 1][j][3])};
      __builtin_amdgcn_raw_buffer_store_b128(v, rs, base + lane * 16, 0, 16);
      if (cnt == 3) {
        u32x2 v2 = {pack2s(acc[i0 + 2][j][0], acc[i0 + 2][j][1]), pack2s(acc[i0 + 2][j][2], acc[i0 + 2][j][3])};
        __builtin_amdgcn_raw_buffer_store_b64(v2, rs, base + 1024 + lane * 8, 0, 16);
      }
    }
  }
  const int jm = kq >> 1, i0 = (kq & 1) ? HALF : 0, cnt = (kq & 1) ? NR - HALF : HALF;
  const unsigned short* h = (const unsigned short*)(ws + WS_H);
  const int s = ntile * 128 + (ct0 + jm) * 16 + r;
  u32x2 hres[3];
#pragma unroll
  for (int k = 0; k < 3; ++k) {
    const int i = i0 + (k < cnt ? k : 0);
    hres[k] = *(const u32x2*)(h + (size_t)s * D_ + mblk * 144 + (rt0 + i) * 16 + 4 * g);
  }
  const float bias = __uint_as_float(((unsigned)b2[s]) << 16);
  f32x4 mine[3];
  if (kq == 0) { mine[0] = acc[0][0]; mine[1] = acc[1][0]; mine[2] = acc[2][0]; }
  else if (kq == 1) { mine[0] = acc[HALF][0]; mine[1] = acc[HALF + 1][0]; mine[2] = acc[0][0]; }
  else if (kq == 2) { mine[0] = acc[0][1]; mine[1] = acc[1][1]; mine[2] = acc[2][1]; }
  else { mine[0] = acc[HALF][1]; mine[1] = acc[HALF + 1][1]; mine[2] = acc[0][1]; }
  PROF(4);
  u32x4 pa[3]; u32x2 pb[3];
  const char* inbox_a = ws + WS_P5 + (size_t)(tile * 4 + kq) * P5_SLOT + w * 3 * 1536 + lane * 16;
  const char* inbox_b = ws + WS_P5 + (size_t)(tile * 4 + kq) * P5_SLOT + w * 3 * 1536 + 1024 + lane * 8;
#pragma unroll
  for (int p = 0; p < 3; ++p) pb[p] = u32x2{0u, 0u};
  for (unsigned spin = 0; spin < K5_CAP; ++spin) {
    if (cnt == 3)
      asm volatile("global_load_dwordx4 %0, %6, off sc1\n\tglobal_load_dwordx4 %1, %6, off offset:1536 sc1\n\tglobal_load_dwordx4 %2, %6, off offset:3072 sc1\n\t"
                   "global_load_dwordx2 %3, %7, off sc1\n\tglobal_load_dwordx2 %4, %7, off offset:1536 sc1\n\tglobal_load_dwordx2 %5, %7, off offset:3072 sc1\n\t"
                   "s_waitcnt vmcnt(0)"
                   : "=&v"(pa[0]), "=&v"(pa[1]), "=&v"(pa[2]), "=&v"(pb[0]), "=&v"(pb[1]), "=&v"(pb[2]) : "v"(inbox_a), "v"(inbox_b) : "memory");
    else
      asm volatile("global_load_dwordx4 %0, %3, off sc1\n\tglobal_load_dwordx4 %1, %3, off offset:1536 sc1\n\tglobal_load_dwordx4 %2, %3, off offset:3072 sc1\n\t"
                   "s_waitcnt vmcnt(0)"
                   : "=&v"(pa[0]), "=&v"(pa[1]), "=&v"(pa[2]) : "v"(inbox_a) : "memory");
    unsigned all = 0u;
#pragma unroll
    for (int p = 0; p < 3; ++p)
      all |= (pa[p][0] == 0xffffffffu) | (pa[p][1] == 0xffffffffu) | (pa[p][2] == 0xffffffffu) | (pa[p][3] == 0xffffffffu) |
             (pb[p][0] == 0xffffffffu) | (pb[p][1] == 0xffffffffu);
    if (!__any(all != 0u)) break;
    __builtin_amdgcn_s_sleep(2);
  }
  PROF(5);
#pragma unroll
  for (int p = 0; p < 3; ++p) {
    mine[0] += f32x4{bflo(pa[p][0]), bfhi(pa[p][0]), bflo(pa[p][1]), bfhi(pa[p][1])};
    mine[1] += f32x4{bflo(pa[p][2]), bfhi(pa[p][2]), bflo(pa[p][3]), bfhi(pa[p][3])};
    mine[2] += f32x4{bflo(pb[p][0]), bfhi(pb[p][0]), bflo(pb[p][1]), bfhi(pb[p][1])};
  }
#pragma unroll
  for (int k = 0; k < 3; ++k) {
    if (k < cnt) {
      const f32x4 v = mine[k];
      const int d = mblk * 144 + (rt0 + i0 + k) * 16 + 4 * g;
      u32x2 o = {pack2(bflo(hres[k][0]) + (v[0] + bias), bfhi(hres[k][0]) + (v[1] + bias)),
                 pack2(bflo(hres[k][1]) + (v[2] + bias), bfhi(hres[k][1]) + (v[3] + bias))};
      *(u32x2*)(out + (size_t)s * D_ + d) = o;
    }
  }
}

__global__ __launch_bounds__(768) void k5_gemm2(const unsigned short* __restrict__ w2, const unsigned short* __restrict__ b2,
                                                char* __restrict__ ws, unsigned short* __restrict__ out) {
  __shared__ __attribute__((aligned(16))) char smem[C5::NSLOT * C5::SLOT];
  const int b = blockIdx.x, xg = b & 7, idx = b >> 3;
  const int kq = xg >> 1, ntile = (xg & 1) * 8 + (idx & 7), mblk = idx >> 3;
  const int tile = mblk * 16 + ntile;
  const int tid = threadIdx.x, lane = tid & 63;
  const int w = __builtin_amdgcn_readfirstlane(tid >> 6);
  const unsigned sbase = lds_addr(smem);
  const int g = lane >> 4;
  PROF_DECL PROF(0);
  if (w >= 8) {
    const int j = w - 8;
    unsigned oa[5], ob[4];
#pragma unroll
    for (int i = 0; i < 4; ++i) oa[i] = (i * 4 + j) * 1024 + lane * 16;
    oa[4] = 16384 + j * 512 + (lane & 31) * 16;
#pragma unroll
    for (int i = 0; i < 4; ++i) {
      const int rho = (i * 4 + j) * 4 + (lane >> 4), cc = (lane & 15) ^ ((rho & 7) << 1);
      ob[i] = swap23(rho) * (S_ * 2) + cc * 16;
    }
    gemm_loader<C5>(sbase, j, lane, ws + WS_U + ((size_t)mblk * 144 + kq * 36) * 9216, (const char*)(w2 + (size_t)(kq * 1152) * S_ + ntile * 128), oa, ob, 0);
    return;
  }
  const int q4 = (lane & 15) >> 2, p4 = lane & 3;
  const int rho = 16 * (g >> 1) + 4 * (g & 1) + q4;
  const int wr = w >> 2, wc = w & 3;
  unsigned bb[2];
#pragma unroll
  for (int j = 0; j < 2; ++j) bb[j] = sbase + C5::ABYTES + rho * 256 + (((wc * 2 + j) ^ (rho & 7)) * 32) + p4 * 8;
  f32x4 acc[5][2], st[5];
#pragma unroll
  for (int i = 0; i < 5; ++i)
#pragma unroll
    for (int j = 0; j < 2; ++j) acc[i][j] = f32x4{0, 0, 0, 0};
  PROF(1);
  if (wr == 0) { gemm_core<C5, 5, 2, 0>(sbase, lane, 0, bb, acc, st PROF_PASS); PROF(2); k5_epilogue<5>(tile, kq, w, lane, mblk, ntile, 0, 2 * wc, acc, b2, ws, out PROF_PASS); }
  else { gemm_core<C5, 4, 2, 0>(sbase, lane, 5, bb, acc, st PROF_PASS); PROF(2); k5_epilogue<4>(tile, kq, w, lane, mblk, ntile, 5, 2 * wc, acc, b2, ws, out PROF_PASS); }
  PROF(3); PROF_FLUSH(5);
}

extern "C" void kernel_launch(void* const* d_in, const int* in_sizes, int n_in, void* d_out, int out_size, void* d_ws, size_t ws_size,
                              hipStream_t stream) {
  const unsigned short* x = (const unsigned short*)d_in[0];
  const unsigned short* wqkv = (const unsigned short*)d_in[1];
  const unsigned short* wo = (const unsigned short*)d_in[2];
  const unsigned short* w1 = (const unsigned short*)d_in[4];
  const unsigned short* b1 = (const unsigned short*)d_in[5];
  const unsigned short* w2 = (const unsigned short*)d_in[6];
  const unsigned short* b2 = (const unsigned short*)d_in[7];
  char* ws = (char*)d_ws;
  k1_qkv<<<256, 768, 0, stream>>>(x, wqkv, ws);
  k2_attn<<<256, 768, 0, stream>>>(ws, wo, w1, w2);
  k3_oproj<<<256, 768, 0, stream>>>(x, ws);
  k4_gemm1<<<256, 768, 0, stream>>>(w1, b1, w2, ws);
  k5_gemm2<<<256, 768, 0, stream>>>(w2, b2, ws, (unsigned short*)d_out);
}
```
